```python
import torch

def _softmax(x, axis=-1):
    return torch.softmax(x, dim=axis)
def _gelu(x):
    return torch.nn.functional.gelu(x, approximate="tanh")
def _silu(x):
    return torch.nn.functional.silu(x)
def _sigmoid(x):
    return torch.sigmoid(x)
def _layernorm(x, axis=-1, eps=1e-5):
    m = x.mean(dim=axis, keepdim=True)
    v = x.var(dim=axis, keepdim=True, unbiased=False)
    return (x - m) / torch.sqrt(v + eps)
def _scatter_add(tgt, idx, src, axis=0):
    return tgt.clone().index_add_(axis, idx, src)
def _segment_sum(src, seg, n):
    out = torch.zeros((n,) + src.shape[1:], dtype=src.dtype)
    return out.index_add_(0, seg, src)
def _pad(x, pw):
    flat = tuple(v for pair in reversed(pw) for v in pair)
    return torch.nn.functional.pad(x, flat)

kernel_name = 'standalone__sliding_block_cached__sampling'

def setup_inputs():

    _w = (lambda a: (lambda t: t.to(torch.bfloat16) if t.is_floating_point() else t)(torch.as_tensor(a)))
    B, Sq, D = 8, 32, 1024
    Hq, Hkv, Sk, Dh = 32, 4, 8192, 128
    assert Hq % Hkv == 0, (Hq, Hkv)
    return {
        "x": _w((lambda *s: torch.randn(*s, dtype=torch.bfloat16) * 0.8)(B, Sq, D)),
        "k_cache": _w((lambda *s: torch.randn(*s, dtype=torch.bfloat16) * 0.8)(B, Hkv, Sk, Dh)),
        "v_cache": _w((lambda *s: torch.randn(*s, dtype=torch.bfloat16) * 0.05)(B, Hkv, Sk, Dh)),
        "wq": _w((lambda *s: torch.randn(*s, dtype=torch.bfloat16) * 0.8)(D, Hq * Dh) * (D ** -0.5)),
        "wo": _w((lambda *s: torch.randn(*s, dtype=torch.bfloat16) * 0.05)(Hq * Dh, D) * ((Hq * Dh) ** -0.5)),
    }

def reference(x, k_cache, v_cache, wq, wo):
    B, Sq, D = x.shape
    Hq, Dh = 32, 128
    Hkv, Sk = k_cache.shape[1], k_cache.shape[2]
    W = min(4096, max(256, Sk // 4))
    rep = Hq // Hkv
    q = (lambda x, *d: x.permute(*d))((x @ wq).reshape(B, Sq, Hq, Dh), 0, 2, 1, 3)
    k = (lambda x, n, axis: torch.repeat_interleave(x, n, dim=axis))(k_cache[:, :, Sk - W:, :], rep, axis=1)
    v = (lambda x, n, axis: torch.repeat_interleave(x, n, dim=axis))(v_cache[:, :, Sk - W:, :], rep, axis=1)
    scores = torch.einsum("bhqd,bhkd->bhqk", q, k) * (Dh ** -0.5)
    p = _softmax(scores, axis=-1)
    o = torch.einsum("bhqk,bhkd->bhqd", p, v)
    return (lambda x, *d: x.permute(*d))(o, 0, 2, 1, 3).reshape(B, Sq, Hq * Dh) @ wo
```

```cpp
#include <hip/hip_runtime.h>
#include <hip/hip_bf16.h>
typedef unsigned short u16;
__device__ __forceinline__ float b2f(u16 v){ return __uint_as_float(((unsigned)v)<<16); }
__device__ __forceinline__ u16 f2b(float f){ unsigned u=__float_as_uint(f); u += 0x7fff + ((u>>16)&1); return (u16)(u>>16); }
__global__ void gemm(const u16* A, const u16* B, u16* C, int M, int N, int K){
  int n = blockIdx.x*64 + (threadIdx.x&63); int m = blockIdx.y*4 + (threadIdx.x>>6);
  float acc=0.f;
  for(int k=0;k<K;++k) acc += b2f(A[(size_t)m*K+k])*b2f(B[(size_t)k*N+n]);
  C[(size_t)m*N+n]=f2b(acc);
}
__global__ void attn(const u16* q, const u16* kc, const u16* vc, u16* o){
  int s = blockIdx.x&31, h=(blockIdx.x>>5)&31, b=blockIdx.x>>10;
  __shared__ float qs[128]; __shared__ float p[2048]; __shared__ float red[256];
  int t=threadIdx.x;
  if(t<128) qs[t]=b2f(q[(size_t)(b*32+s)*4096 + h*128 + t]);
  __syncthreads();
  const u16* K = kc + ((size_t)(b*4 + h/8)*8192 + 6144)*128;
  const u16* V = vc + ((size_t)(b*4 + h/8)*8192 + 6144)*128;
  float mx=-1e30f;
  for(int k=t;k<2048;k+=256){ float a=0; for(int d=0;d<128;++d) a+=qs[d]*b2f(K[(size_t)k*128+d]); a*=0.08838834764831845f; p[k]=a; mx=fmaxf(mx,a);}
  red[t]=mx; __syncthreads();
  for(int o2=128;o2>0;o2>>=1){ if(t<o2) red[t]=fmaxf(red[t],red[t+o2]); __syncthreads(); }
  mx=red[0]; __syncthreads();
  float sm=0; for(int k=t;k<2048;k+=256){ float e=__expf(p[k]-mx); p[k]=e; sm+=e; }
  red[t]=sm; __syncthreads();
  for(int o2=128;o2>0;o2>>=1){ if(t<o2) red[t]+=red[t+o2]; __syncthreads(); }
  sm=red[0];
  if(t<128){ float a=0; for(int k=0;k<2048;++k) a+=p[k]*b2f(V[(size_t)k*128+t]); o[(size_t)(b*32+s)*4096+h*128+t]=f2b(a/sm); }
}
extern "C" void kernel_launch(void* const* d_in, const int* in_sizes, int n_in, void* d_out, int out_size, void* d_ws, size_t ws_size, hipStream_t stream){
  const u16* x=(const u16*)d_in[0]; const u16* kc=(const u16*)d_in[1]; const u16* vc=(const u16*)d_in[2]; const u16* wq=(const u16*)d_in[3]; const u16* wo=(const u16*)d_in[4];
  u16* q=(u16*)d_ws; u16* o=q+256*4096;
  gemm<<<dim3(64,64),256,0,stream>>>(x,wq,q,256,4096,1024);
  attn<<<8192,256,0,stream>>>(q,kc,vc,o);
  gemm<<<dim3(16,64),256,0,stream>>>(o,wo,(u16*)d_out,256,1024,4096);
}
```

```cpp
#include <hip/hip_runtime.h>

typedef unsigned u32;
typedef unsigned long long u64;
typedef u32 u32x2 __attribute__((ext_vector_type(2)));
typedef u32 u32x4 __attribute__((ext_vector_type(4)));
typedef float f32x2 __attribute__((ext_vector_type(2)));
typedef float f32x4 __attribute__((ext_vector_type(4)));
typedef float f32x16 __attribute__((ext_vector_type(16)));
typedef __bf16 bf16x2 __attribute__((ext_vector_type(2)));
typedef __bf16 bf16x4 __attribute__((ext_vector_type(4)));
typedef __bf16 bf16x8 __attribute__((ext_vector_type(8)));
typedef __amdgpu_buffer_rsrc_t rsrc_t;
#define LAS __attribute__((address_space(3)))
#define DI __device__ __forceinline__
#define PIN() __builtin_amdgcn_sched_barrier(0)

#define KV_NT 0
#define STAMPS 0

constexpr u32 WS_FLAG = 0;
constexpr u32 WS_STAT = 1u << 17;
constexpr u32 WS_Q = 1u << 20;
constexpr u32 WS_P = 4u << 20;
constexpr u32 WS_T = 12u << 20;
constexpr u32 WS_DBG = 20u << 20;
constexpr u32 WS_END = 21u << 20;
constexpr u32 SLOT = 32768;
constexpr u32 SPIN_CAP = 1u << 18;
constexpr u32 CODE_LINES = 128;

DI u32 cvtpk(float lo, float hi) { f32x2 v = {lo, hi}; return __builtin_bit_cast(u32, __builtin_convertvector(v, bf16x2)); }
DI float max2f(float a, float b) { return __builtin_elementwise_maximum(a, b); }
DI float max3f(float a, float b, float c) { return max2f(max2f(a, b), c); }
DI float bflo(u32 x) { return __uint_as_float(x << 16); }
DI float bfhi(u32 x) { return __uint_as_float(x & 0xffff0000u); }
template <int N> DI void waitvm() { asm volatile("s_waitcnt vmcnt(%0)" ::"i"(N) : "memory"); }
DI void bar() { asm volatile("s_waitcnt lgkmcnt(0)\n\ts_barrier" ::: "memory"); }
template <int N> DI void barvm() { asm volatile("s_waitcnt vmcnt(%0) lgkmcnt(0)\n\ts_barrier" ::"i"(N) : "memory"); }
template <int IMM, int NT> DI void dma(u32 m0v, u32 voff, rsrc_t rs, u32 soff) {
  if constexpr (NT) asm volatile("s_mov_b32 m0, %0\n\ts_nop 0\n\tbuffer_load_dwordx4 %1, %2, %3 offen offset:%4 nt lds" ::"s"(m0v), "v"(voff), "s"(rs), "s"(soff), "i"(IMM) : "memory");
  else asm volatile("s_mov_b32 m0, %0\n\ts_nop 0\n\tbuffer_load_dwordx4 %1, %2, %3 offen offset:%4 lds" ::"s"(m0v), "v"(voff), "s"(rs), "s"(soff), "i"(IMM) : "memory");
}
template <int IMM> DI void ld16(u32x4& d, u32 voff, rsrc_t rs, u32 soff) {
  asm volatile("buffer_load_dwordx4 %0, %1, %2, %3 offen offset:%4 sc1" : "=v"(d) : "v"(voff), "s"(rs), "s"(soff), "i"(IMM) : "memory");
}
template <int IMM> DI void ld4(u32& d, u32 voff, rsrc_t rs, u32 soff) {
  asm volatile("buffer_load_dword %0, %1, %2, %3 offen offset:%4 sc1" : "=v"(d) : "v"(voff), "s"(rs), "s"(soff), "i"(IMM) : "memory");
}
template <int IMM> DI void st16(u32x4 v, u32 voff, rsrc_t rs, u32 soff) {
  asm volatile("buffer_store_dwordx4 %0, %1, %2, %3 offen offset:%4 sc1\n\ts_nop 1" ::"v"(v), "v"(voff), "s"(rs), "s"(soff), "i"(IMM) : "memory");
}
DI void st4(u32 v, u32 voff, rsrc_t rs, u32 soff) { __builtin_amdgcn_raw_buffer_store_b32(v, rs, voff, soff, 16); }
DI void poll(u32 voff, rsrc_t rs, u32 soff) {
  for (u32 n = 0; n < SPIN_CAP; ++n) {
    u32 v;
    asm volatile("buffer_load_dword %0, %1, %2, %3 offen sc1\n\ts_waitcnt vmcnt(0)" : "=&v"(v) : "v"(voff), "s"(rs), "s"(soff) : "memory");
    if (__all(v == 1u)) break;
  }
}
DI bf16x8 ldsb128(u32 a) { return *(const LAS bf16x8*)(size_t)a; }
DI bf16x8 ldstr(u32 a0, u32 a1) {
  bf16x4 lo = __builtin_amdgcn_ds_read_tr16_b64_v4bf16((LAS bf16x4*)(size_t)a0);
  bf16x4 hi = __builtin_amdgcn_ds_read_tr16_b64_v4bf16((LAS bf16x4*)(size_t)a1);
  return __builtin_shufflevector(lo, hi, 0, 1, 2, 3, 4, 5, 6, 7);
}
DI rsrc_t mkrs(const void* p, u32 bytes) { return __builtin_amdgcn_make_buffer_rsrc((void*)p, 0, bytes, 0x00020000); }

#define STAMP(i)

__global__ __launch_bounds__(512, 2) void fused_kernel(const void* __restrict__ px, const void* __restrict__ pk, const void* __restrict__ pv,
                                                       const void* __restrict__ pwq, const void* __restrict__ pwo, void* __restrict__ pout,
                                                       void* __restrict__ pws) {
  extern __shared__ __attribute__((aligned(16))) char lds_raw[];
  STAMP(0);
  const u32 tid = threadIdx.x, lane = tid & 63;
  u32 w = __builtin_amdgcn_readfirstlane(tid >> 6);
  asm volatile("s_nop 4" : "+s"(w));
  const u32 bid = blockIdx.x, xcd = bid & 7, jj = bid >> 3;
  const u32 kvh = xcd & 3, b = (xcd >> 2) * 4 + (jj >> 3), s = jj & 3, quad = kvh * 2 + ((jj >> 2) & 1);
  const u32 bp = b >> 1, par = b & 1, e = par * 4 + s, L = (b * 8 + quad) * 4 + s;
  const u32 wl = w & 3;
  const bool isB = w >= 4;
  const u32 lb = (u32)(size_t)(LAS char*)lds_raw;

  u32 pf;
  {
    u64 pc;
    asm volatile("s_getpc_b64 %0" : "=s"(pc));
    const u32 ln = (jj & 3) * 64 + w * 8 + (lane & 7);
    const char* cp = (const char*)(pc & ~127ull) + (ln < CODE_LINES ? ln : CODE_LINES - 1) * 128u;
    asm volatile("global_load_dword %0, %1, off" : "=v"(pf) : "v"(cp) : "memory");
  }
  const rsrc_t rx = mkrs(px, 256u * 1024 * 2), rk = mkrs(pk, 8u * 4 * 8192 * 256), rv = mkrs(pv, 8u * 4 * 8192 * 256);
  const rsrc_t rq = mkrs(pwq, 1024u * 4096 * 2), ro = mkrs(pwo, 4096u * 1024 * 2), rw = mkrs(pws, WS_END), rout = mkrs(pout, 256u * 1024 * 2);
  if (w == 0) { u32x2 z = {0u, 0u}; __builtin_amdgcn_raw_buffer_store_b64(z, rw, lane * 8, WS_FLAG + L * 512, 16); }

  const u32 l15 = lane & 15, g4 = lane >> 4, r32 = lane & 31, h2 = lane >> 5, q4 = l15 >> 2, p4 = lane & 3;

  const u32 kvbase = ((b * 4 + kvh) * 8192 + 6144 + s * 512) * 256 + wl * 8192;
  const u32 vK = g4 * 256 + ((l15 ^ g4) << 4);
  const u32 vV = g4 * 256 + ((l15 ^ (g4 << 2)) << 4);
  u32x4 qfrag;
  {
    const u32 xr0 = 8 * w + g4, xr1 = xr0 + 4;
    const u32 vX0 = xr0 * 2048 + ((l15 ^ (xr0 & 15)) << 4), vX1 = xr1 * 2048 + ((l15 ^ (xr1 & 15)) << 4) - 1024;
    const u32 wr = 16 * w + (lane >> 3), f0 = (wr >> 1) & 1;
    const u32 vW0 = wr * 8192 + (((lane & 7) ^ (f0 << 1)) << 4), vW1 = (wr + 8) * 8192 + (((lane & 7) ^ ((f0 | 2) << 1)) << 4) - 1024;
    const u32 sX = bp * 64 * 2048, sW = (quad * 512 + e * 64) * 2;
    const u32 mX = lb + w * 2048, mW = lb + 16384 + w * 2048;
#define QCHUNK(c_, slot_) do { dma<0, 0>(mX + (slot_), vX0, rx, sX + (c_) * 256); dma<1024, 0>(mX + (slot_), vX1, rx, sX + (c_) * 256); \
      dma<0, 0>(mW + (slot_), vW0, rq, sW + (c_) * (128 * 8192)); dma<1024, 0>(mW + (slot_), vW1, rq, sW + (c_) * (128 * 8192)); } while (0)
    QCHUNK(0, 2 * SLOT);
    QCHUNK(1, 3 * SLOT);
    const u32 dt = wl, rt = w >> 2;
    const u32 aA = lb + 16384 + (8 * g4 + q4) * 128 + ((dt ^ (((q4 >> 1) & 1) | ((g4 & 1) << 1))) << 5) + p4 * 8;
    const u32 aB = lb + (rt * 32 + l15) * 256 + ((g4 ^ l15) << 4);
    f32x4 acc0 = {0.f, 0.f, 0.f, 0.f}, acc1 = {0.f, 0.f, 0.f, 0.f};
    u32 rs_ = 2 * SLOT, ws_ = 4 * SLOT;
    asm volatile("s_waitcnt vmcnt(4)" : "+v"(pf)::"memory");
#pragma unroll 1
    for (u32 c = 0; c < 8; ++c) {
      if (c < 7) barvm<4>();
      else if (isB) barvm<8>();
      else barvm<0>();
      if (c < 6) QCHUNK(c + 2, ws_);
      else if (c == 6 && isB) {
        const u32 m = lb + wl * 8192;
        dma<0, KV_NT>(m, vK, rk, kvbase); dma<1024, KV_NT>(m, vK ^ 64, rk, kvbase); dma<2048, KV_NT>(m, vK ^ 128, rk, kvbase); dma<3072, KV_NT>(m, vK ^ 192, rk, kvbase);
        dma<0, KV_NT>(m + 4096, vK, rk, kvbase + 4096); dma<1024, KV_NT>(m + 4096, vK ^ 64, rk, kvbase + 4096);
        dma<2048, KV_NT>(m + 4096, vK ^ 128, rk, kvbase + 4096); dma<3072, KV_NT>(m + 4096, vK ^ 192, rk, kvbase + 4096);
      }
      const u32 a = aA + rs_;
#pragma unroll
      for (int ks = 0; ks < 4; ++ks) {
        const bf16x8 fa = ldstr(a + ks * 4096, a + ks * 4096 + 512);
        const u32 bb = (aB ^ (ks * 64)) + rs_;
        const bf16x8 f0_ = ldsb128(bb), f1_ = ldsb128(bb + 4096);
        acc0 = __builtin_amdgcn_mfma_f32_16x16x32_bf16(fa, f0_, acc0, 0, 0, 0);
        acc1 = __builtin_amdgcn_mfma_f32_16x16x32_bf16(fa, f1_, acc1, 0, 0, 0);
      }
      rs_ = rs_ == 4 * SLOT ? 0 : rs_ + SLOT;
      ws_ = ws_ == 4 * SLOT ? 0 : ws_ + SLOT;
    }
#undef QCHUNK
    const float cs = 0.12751743082459868f;
    const auto s0 = __builtin_amdgcn_permlane16_swap(cvtpk(acc0[0] * cs, acc0[1] * cs), cvtpk(acc1[0] * cs, acc1[1] * cs), false, false);
    const auto s1 = __builtin_amdgcn_permlane16_swap(cvtpk(acc0[2] * cs, acc0[3] * cs), cvtpk(acc1[2] * cs, acc1[3] * cs), false, false);
    qfrag = u32x4{s0[0], s1[0], s0[1], s1[1]};
  }
  STAMP(1);
  const u32 cflag_v = lane < 4 ? lane * 512 : 0x7fffffffu;
  const u32 cflag_s = WS_FLAG + (((bp * 2 + (w >> 2)) * 8 + quad) * 4) * 512 + (e * 4 + wl) * 4;
  st16<0>(qfrag, lane * 16, rw, WS_Q + ((((bp * 2 + (w >> 2)) * 32 + quad * 4) * 8 + e * 4 + wl) << 10));
  bf16x8 qf[8];
  if (isB) {
#pragma unroll 1
    for (u32 hc = 2; hc < 8; ++hc) {
      const u32 off = (hc >> 1) * SLOT + (hc & 1) * 4096, m = lb + wl * 8192 + off;
      waitvm<7>(); dma<0, KV_NT>(m, vK, rk, kvbase + off);
      waitvm<7>(); dma<1024, KV_NT>(m, vK ^ 64, rk, kvbase + off);
      waitvm<7>(); dma<2048, KV_NT>(m, vK ^ 128, rk, kvbase + off);
      waitvm<7>();
      if (hc == 3) st4(1u, cflag_v, rw, cflag_s);
      dma<3072, KV_NT>(m, vK ^ 192, rk, kvbase + off);
      if (hc == 5) barvm<8>();
    }
#pragma unroll 1
    for (u32 hc = 0; hc < 2; ++hc) {
      const u32 m = lb + 4 * SLOT + wl * 8192 + hc * 4096, so = kvbase + hc * 4096;
      waitvm<7>(); dma<0, KV_NT>(m, vV, rv, so);
      waitvm<7>(); dma<1024, KV_NT>(m, vV, rv, so);
      waitvm<7>(); dma<2048, KV_NT>(m, vV, rv, so);
      waitvm<7>(); dma<3072, KV_NT>(m, vV, rv, so);
    }
    STAMP(2);
    bar();
    barvm<8>();
#pragma unroll
    for (int d = 0; d < 8; ++d) qf[d] = ldsb128(lb + wl * 8192 + d * 1024 + lane * 16);
  } else {
    waitvm<0>();
    st4(1u, cflag_v, rw, cflag_s);
    bar();
    poll((wl * 8 + (lane & 7)) * 4, rw, WS_FLAG + L * 512);
    STAMP(2);
    u32x4 t[8];
    const u32 so = WS_Q + (((b * 32 + quad * 4 + wl) * 8) << 10);
    ld16<0>(t[0], lane * 16, rw, so); ld16<1024>(t[1], lane * 16, rw, so); ld16<2048>(t[2], lane * 16, rw, so); ld16<3072>(t[3], lane * 16, rw, so);
    ld16<0>(t[4], lane * 16, rw, so + 4096); ld16<1024>(t[5], lane * 16, rw, so + 4096); ld16<2048>(t[6], lane * 16, rw, so + 4096); ld16<3072>(t[7], lane * 16, rw, so + 4096);
    asm volatile("s_waitcnt vmcnt(0)" : "+v"(t[0]), "+v"(t[1]), "+v"(t[2]), "+v"(t[3]), "+v"(t[4]), "+v"(t[5]), "+v"(t[6]), "+v"(t[7])::"memory");
#pragma unroll
    for (int d = 0; d < 8; ++d) qf[d] = __builtin_bit_cast(bf16x8, t[d]);
  }
  STAMP(3);
  f32x16 S[8];
  float mx;
  {
    const u32 aK = lb + (isB ? 2 * SLOT : 0) + r32 * 256 + ((h2 ^ l15) << 4);
    float m0 = -__builtin_inff(), m1 = -__builtin_inff();
    constexpr int PD = 6;
    bf16x8 kf[PD];
#pragma unroll
    for (int n = 0; n < PD; ++n) kf[n] = ldsb128((aK ^ ((n & 7) * 32)) + (n >> 3) * 8192);
    PIN();
#pragma unroll
    for (int n = 0; n < 64; ++n) {
      const int t = n >> 3, d = n & 7;
      if (d == 0) S[t] = f32x16{};
      S[t] = __builtin_amdgcn_mfma_f32_32x32x16_bf16(kf[n % PD], qf[d], S[t], 0, 0, 0);
      if (n + PD < 64) kf[n % PD] = ldsb128((aK ^ (((n + PD) & 7) * 32)) + ((n + PD) >> 3) * 8192);
      if (t > 0 && d >= 4) { const int i = (d - 4) * 4; m0 = max3f(m0, S[t - 1][i], S[t - 1][i + 1]); m1 = max3f(m1, S[t - 1][i + 2], S[t - 1][i + 3]); asm volatile("" : "+v"(m0), "+v"(m1)); }
      PIN();
      if (n == 31) {
        asm volatile("s_barrier" ::: "memory");
        if (!isB) {
#pragma unroll
          for (int dd = 0; dd < 8; ++dd) *(LAS bf16x8*)(size_t)(lb + wl * 8192 + dd * 1024 + lane * 16) = qf[dd];
        }
        PIN();
      }
    }
    barvm<0>();
    PIN();
#pragma unroll
    for (int i = 0; i < 16; i += 4) { m0 = max3f(m0, S[7][i], S[7][i + 1]); m1 = max3f(m1, S[7][i + 2], S[7][i + 3]); }
    mx = max2f(m0, m1);
    const auto sw = __builtin_amdgcn_permlane32_swap(__float_as_uint(mx), __float_as_uint(mx), false, false);
    mx = max2f(__uint_as_float(sw[0]), __uint_as_float(sw[1]));
  }
  STAMP(4);
  st4(1u, cflag_v, rw, cflag_s);
  u32x4 P[16];
  float ls, mA = 0.f, lA = 0.f;
  {
    float l0 = 0.f, l1 = 0.f, l2 = 0.f, l3 = 0.f;
#pragma unroll
    for (int hf = 0; hf < 2; ++hf) {
      const u32 ch = isB ? 3 : 1 + hf, m = lb + (isB ? 2 : 1 - hf) * SLOT + wl * 8192, so = kvbase + ch * SLOT;
      const bool en = !(isB && hf == 1);
      if (hf == 1 && isB) { mA = *(const LAS float*)(size_t)(lb + 3 * SLOT + wl * 256 + r32 * 4); lA = *(const LAS float*)(size_t)(lb + 3 * SLOT + wl * 256 + 128 + r32 * 4); }
#pragma unroll
      for (int u = 0; u < 8; ++u) {
        if (en) {
          if ((u & 3) == 0) dma<0, KV_NT>(m + (u >> 2) * 4096, vV, rv, so + (u >> 2) * 4096);
          if ((u & 3) == 1) dma<1024, KV_NT>(m + (u >> 2) * 4096, vV, rv, so + (u >> 2) * 4096);
          if ((u & 3) == 2) dma<2048, KV_NT>(m + (u >> 2) * 4096, vV, rv, so + (u >> 2) * 4096);
          if ((u & 3) == 3) dma<3072, KV_NT>(m + (u >> 2) * 4096, vV, rv, so + (u >> 2) * 4096);
        }
        PIN();
        const int t = hf * 4 + (u >> 1), o = (u & 1) * 8;
        float p[8];
#pragma unroll
        for (int i = 0; i < 8; ++i) p[i] = __builtin_amdgcn_exp2f(S[t][o + i] - mx);
        l0 += p[0]; l1 += p[1]; l2 += p[2]; l3 += p[3]; l0 += p[4]; l1 += p[5]; l2 += p[6]; l3 += p[7];
        P[hf * 8 + u] = u32x4{cvtpk(p[0], p[1]), cvtpk(p[2], p[3]), cvtpk(p[4], p[5]), cvtpk(p[6], p[7])};
        asm volatile("" : "+v"(P[hf * 8 + u]), "+v"(l0), "+v"(l1), "+v"(l2), "+v"(l3));
        PIN();
      }
      bar();
    }
    ls = (l0 + l1) + (l2 + l3);
    const auto sw = __builtin_amdgcn_permlane32_swap(__float_as_uint(ls), __float_as_uint(ls), false, false);
    ls = __uint_as_float(sw[0]) + __uint_as_float(sw[1]);
  }
  STAMP(5);
  if (!isB) *(LAS float*)(size_t)(lb + 3 * SLOT + wl * 256 + lane * 4) = h2 ? ls : mx;
  f32x16 O[4] = {};
  f32x16 Dm[4];
  float fB = 0.f, stat = 0.f;
  {
    const u32 aV = lb + (4 * h2 + q4) * 256 + (q4 << 6) + (g4 & 1) * 32 + p4 * 8;
#pragma unroll
    for (int hf = 0; hf < 2; ++hf) {
      const u32 vb = (isB ? 2 * hf : 4 - 3 * hf) * SLOT;
      constexpr int PD = 6;
      bf16x8 vf[PD];
#pragma unroll
      for (int n = 0; n < PD; ++n) { const u32 a = (aV ^ ((n & 3) << 6)) + vb + (n >> 2) * 4096; vf[n] = ldstr(a, a + 2048); }
      PIN();
#pragma unroll
      for (int n = 0; n < 32; ++n) {
        O[n & 3] = __builtin_amdgcn_mfma_f32_32x32x16_bf16(vf[n % PD], __builtin_bit_cast(bf16x8, P[hf * 8 + (n >> 2)]), O[n & 3], 0, 0, 0);
        if (n + PD < 32) { const u32 a = (aV ^ (((n + PD) & 3) << 6)) + vb + ((n + PD) >> 2) * 4096; vf[n % PD] = ldstr(a, a + 2048); }
        PIN();
      }
      if (hf == 0) {
        barvm<8>();
        STAMP(6);
        if (isB) {
          const float M = max2f(mA, mx), fa = __builtin_amdgcn_exp2f(mA - M), fb = __builtin_amdgcn_exp2f(mx - M);
          const float Ls = fa * lA + fb * ls, inv = 1.0f / Ls, fA = fa * inv;
          fB = fb * inv;
          stat = M + __builtin_amdgcn_logf(Ls);
          const u32 da = lb + (wl < 2 ? 4 : 1) * SLOT + (wl & 1) * 16384 + lane * 16;
#pragma unroll
          for (int dt = 0; dt < 4; ++dt)
#pragma unroll
            for (int i = 0; i < 4; ++i) {
              const f32x4 v = *(const LAS f32x4*)(size_t)(da + (dt * 4 + i) * 1024);
              Dm[dt][4 * i] = v[0] * fA; Dm[dt][4 * i + 1] = v[1] * fA; Dm[dt][4 * i + 2] = v[2] * fA; Dm[dt][4 * i + 3] = v[3] * fA;
            }
          asm volatile("" : "+v"(Dm[0]), "+v"(Dm[1]), "+v"(Dm[2]), "+v"(Dm[3]));
          barvm<0>();
          STAMP(7);
        }
      }
    }
  }
  STAMP(8);
  if (!isB) {
    barvm<0>();
    const u32 da = lb + (wl < 2 ? 4 : 1) * SLOT + (wl & 1) * 16384 + lane * 16;
#pragma unroll
    for (int dt = 0; dt < 4; ++dt)
#pragma unroll
      for (int i = 0; i < 4; ++i) *(LAS f32x4*)(size_t)(da + (dt * 4 + i) * 1024) = f32x4{O[dt][4 * i], O[dt][4 * i + 1], O[dt][4 * i + 2], O[dt][4 * i + 3]};
    bar();
    const u32 vW0 = h2 * 2048 + ((r32 ^ (h2 << 2)) << 4), vW1 = h2 * 2048 + 3072 + ((r32 ^ ((2 + h2) << 2)) << 4);
    u32 so = ((quad * 4 + par * 2) * 128 + 16 * wl) * 2048 + s * 512;
#pragma unroll 1
    for (u32 c = 0; c < 4; ++c) {
      const u32 m = lb + ((0x1403u >> (4 * c)) & 7) * SLOT + wl * 8192;
#pragma unroll
      for (int i = 0; i < 4; ++i) {
        waitvm<7>(); dma<0, 0>(m + i * 2048, vW0, ro, so + i * 8192);
        waitvm<7>(); dma<1024, 0>(m + i * 2048, vW1, ro, so + i * 8192);
      }
      so += 64 * 2048;
      if (c == 0) bar();
    }
    STAMP(9);
    barvm<0>();
  } else {
    const u32 hs = (b * 32 + quad * 4 + wl) * 4 + s;
#pragma unroll
    for (int dt = 0; dt < 4; ++dt)
#pragma unroll
      for (int u = 0; u < 2; ++u) {
        float o[8];
#pragma unroll
        for (int i = 0; i < 8; ++i) o[i] = fmaf(O[dt][8 * u + i], fB, Dm[dt][8 * u + i]);
        const u32x4 ov = {cvtpk(o[0], o[1]), cvtpk(o[2], o[3]), cvtpk(o[4], o[5]), cvtpk(o[6], o[7])};
        if (u == 0) st16<0>(ov, lane * 16, rw, WS_P + hs * 8192 + dt * 2048); else st16<1024>(ov, lane * 16, rw, WS_P + hs * 8192 + dt * 2048);
      }
    st4(__float_as_uint(stat), lane < 32 ? lane * 4 : 0x7fffffffu, rw, WS_STAT + hs * 128);
    waitvm<0>();
    st4(1u, cflag_v, rw, WS_FLAG + (((bp * 2 + (wl >> 1)) * 8 + quad) * 4) * 512 + 128 + ((par * 2 + (wl & 1)) * 4 + s) * 4);
    STAMP(9);
    bar();
  }
  STAMP(10);
  f32x16 acc[2] = {};
  {
    poll(((w >> 1) * 4 + p4) * 4, rw, WS_FLAG + L * 512 + 128);
    STAMP(11);
    const u32 hs = ((bp * 2 + (w >> 2)) * 32 + quad * 4 + par * 2 + ((w >> 1) & 1)) * 4;
    u32 st[4];
    u32x4 G[4][4];
    ld4<0>(st[0], r32 * 4, rw, WS_STAT + hs * 128); ld4<128>(st[1], r32 * 4, rw, WS_STAT + hs * 128);
    ld4<256>(st[2], r32 * 4, rw, WS_STAT + hs * 128); ld4<384>(st[3], r32 * 4, rw, WS_STAT + hs * 128);
    const u32 so = WS_P + hs * 8192 + (w & 1) * 4096;
#pragma unroll
    for (int sp = 0; sp < 4; ++sp) ld16<0>(G[0][sp], lane * 16, rw, so + sp * 8192);
#pragma unroll
    for (int sp = 0; sp < 4; ++sp) ld16<1024>(G[1][sp], lane * 16, rw, so + sp * 8192);
#pragma unroll
    for (int sp = 0; sp < 4; ++sp) ld16<2048>(G[2][sp], lane * 16, rw, so + sp * 8192);
#pragma unroll
    for (int sp = 0; sp < 4; ++sp) ld16<3072>(G[3][sp], lane * 16, rw, so + sp * 8192);
    bf16x8 Wf[4][4];
    const u32 p4s = ((p4 & 1) << 1) | (p4 >> 1);
    const u32 aW = lb + (4 * h2 + q4) * 512 + ((w ^ q4) << 6) + (g4 & 1) * 32 + p4s * 8;
#pragma unroll
    for (int c = 0; c < 4; ++c)
#pragma unroll
      for (int i = 0; i < 4; ++i) {
        const u32 a = aW + ((0x1403u >> (4 * c)) & 7) * SLOT + i * 8192;
        Wf[c][i] = ldstr(a, a + 4096);
      }
    asm volatile("s_waitcnt vmcnt(16)" : "+v"(st[0]), "+v"(st[1]), "+v"(st[2]), "+v"(st[3])::"memory");
    float wt[4];
    {
      const float s0 = __uint_as_float(st[0]), s1 = __uint_as_float(st[1]), s2 = __uint_as_float(st[2]), s3 = __uint_as_float(st[3]);
      const float M = max2f(max2f(s0, s1), max2f(s2, s3));
      wt[0] = __builtin_amdgcn_exp2f(s0 - M); wt[1] = __builtin_amdgcn_exp2f(s1 - M); wt[2] = __builtin_amdgcn_exp2f(s2 - M); wt[3] = __builtin_amdgcn_exp2f(s3 - M);
      const float inv = 1.0f / ((wt[0] + wt[1]) + (wt[2] + wt[3]));
      wt[0] *= inv; wt[1] *= inv; wt[2] *= inv; wt[3] *= inv;
    }
#pragma unroll
    for (int i = 0; i < 4; ++i) {
      if (i == 0) asm volatile("s_waitcnt vmcnt(12)" : "+v"(G[0][0]), "+v"(G[0][1]), "+v"(G[0][2]), "+v"(G[0][3])::"memory");
      if (i == 1) asm volatile("s_waitcnt vmcnt(8)" : "+v"(G[1][0]), "+v"(G[1][1]), "+v"(G[1][2]), "+v"(G[1][3])::"memory");
      if (i == 2) asm volatile("s_waitcnt vmcnt(4)" : "+v"(G[2][0]), "+v"(G[2][1]), "+v"(G[2][2]), "+v"(G[2][3])::"memory");
      if (i == 3) asm volatile("s_waitcnt vmcnt(0)" : "+v"(G[3][0]), "+v"(G[3][1]), "+v"(G[3][2]), "+v"(G[3][3])::"memory");
      u32x4 o;
#pragma unroll
      for (int d = 0; d < 4; ++d) {
        float lo = bflo(G[i][0][d]) * wt[0], hi = bfhi(G[i][0][d]) * wt[0];
#pragma unroll
        for (int sp = 1; sp < 4; ++sp) { lo = fmaf(bflo(G[i][sp][d]), wt[sp], lo); hi = fmaf(bfhi(G[i][sp][d]), wt[sp], hi); }
        o[d] = cvtpk(lo, hi);
      }
      *(LAS u32x4*)(size_t)(lb + 2 * SLOT + (i * 8 + w) * 1024 + lane * 16) = o;
      bar();
#pragma unroll
      for (int c = 0; c < 4; ++c)
#pragma unroll
        for (int rb = 0; rb < 2; ++rb)
          acc[rb] = __builtin_amdgcn_mfma_f32_32x32x16_bf16(Wf[c][i], ldsb128(lb + 2 * SLOT + (i * 8 + rb * 4 + c) * 1024 + lane * 16), acc[rb], 0, 0, 0);
    }
  }
  STAMP(12);
  {
    const u32 src = quad * 2 + par, Ld = ((bp * 2) * 8 + w) * 4 + s;
#pragma unroll
    for (int rb = 0; rb < 2; ++rb)
#pragma unroll
      for (int t = 0; t < 2; ++t) {
        const u32x4 ov = {cvtpk(acc[rb][8 * t], acc[rb][8 * t + 1]), cvtpk(acc[rb][8 * t + 2], acc[rb][8 * t + 3]),
                          cvtpk(acc[rb][8 * t + 4], acc[rb][8 * t + 5]), cvtpk(acc[rb][8 * t + 6], acc[rb][8 * t + 7])};
        if (t == 0) st16<0>(ov, (r32 * 2 + h2) * 16, rw, WS_T + ((Ld + 32 * rb) * 16 + src) * 2048);
        else st16<1024>(ov, (r32 * 2 + h2) * 16, rw, WS_T + ((Ld + 32 * rb) * 16 + src) * 2048);
      }
    waitvm<0>();
    const u32 gflag_v = lane < 2 ? lane * (32 * 512) : 0x7fffffffu, gflag_s = WS_FLAG + Ld * 512 + 256 + src * 4;
    st4(1u, gflag_v, rw, gflag_s);
    STAMP(13);
    poll(l15 * 4, rw, WS_FLAG + L * 512 + 256);
    STAMP(14);
    u32x4 T[4];
    const u32 vo = g4 * 2048 + w * 256 + l15 * 16, so = WS_T + L * 32768;
#pragma unroll
    for (int i = 0; i < 4; ++i) ld16<0>(T[i], vo, rw, so + i * 8192);
    st4(1u, gflag_v, rw, gflag_s);
    asm volatile("s_waitcnt vmcnt(1)" : "+v"(T[0]), "+v"(T[1]), "+v"(T[2]), "+v"(T[3])::"memory");
    float v[8];
#pragma unroll
    for (int d = 0; d < 4; ++d) {
      v[2 * d] = (bflo(T[0][d]) + bflo(T[1][d])) + (bflo(T[2][d]) + bflo(T[3][d]));
      v[2 * d + 1] = (bfhi(T[0][d]) + bfhi(T[1][d])) + (bfhi(T[2][d]) + bfhi(T[3][d]));
    }
    float u[4], z[2];
#pragma unroll
    for (int i = 0; i < 4; ++i) {
      const auto sw = __builtin_amdgcn_permlane32_swap(__float_as_uint(v[i]), __float_as_uint(v[i + 4]), false, false);
      u[i] = __uint_as_float(sw[0]) + __uint_as_float(sw[1]);
    }
#pragma unroll
    for (int i = 0; i < 2; ++i) {
      const auto sw = __builtin_amdgcn_permlane16_swap(__float_as_uint(u[i]), __float_as_uint(u[i + 2]), false, false);
      z[i] = __uint_as_float(sw[0]) + __uint_as_float(sw[1]);
    }
    __builtin_amdgcn_raw_buffer_store_b32(cvtpk(z[0], z[1]), rout, (l15 >> 1) * 2048 + (l15 & 1) * 16 + g4 * 4,
                                          (b * 32 + wl * 8) * 2048 + (s * 256 + quad * 32 + (w >> 2) * 16) * 2, 0);
  }
}

extern "C" void kernel_launch(void* const* d_in, const int* in_sizes, int n_in, void* d_out, int out_size, void* d_ws, size_t ws_size,
                              hipStream_t stream) {
  (void)hipFuncSetAttribute((const void*)fused_kernel, hipFuncAttributeMaxDynamicSharedMemorySize, 163840);
  fused_kernel<<<256, 512, 163840, stream>>>(d_in[0], d_in[1], d_in[2], d_in[3], d_in[4], d_out, d_ws);
}
```

```cpp
#define STAMPS 1
#include <hip/hip_runtime.h>

typedef unsigned u32;
typedef unsigned long long u64;
typedef u32 u32x2 __attribute__((ext_vector_type(2)));
typedef u32 u32x4 __attribute__((ext_vector_type(4)));
typedef float f32x2 __attribute__((ext_vector_type(2)));
typedef float f32x4 __attribute__((ext_vector_type(4)));
typedef float f32x16 __attribute__((ext_vector_type(16)));
typedef __bf16 bf16x2 __attribute__((ext_vector_type(2)));
typedef __bf16 bf16x4 __attribute__((ext_vector_type(4)));
typedef __bf16 bf16x8 __attribute__((ext_vector_type(8)));
typedef __amdgpu_buffer_rsrc_t rsrc_t;
#define LAS __attribute__((address_space(3)))
#define DI __device__ __forceinline__
#define PIN() __builtin_amdgcn_sched_barrier(0)

#define KV_NT 0

constexpr u32 WS_FLAG = 0;
constexpr u32 WS_STAT = 1u << 17;
constexpr u32 WS_Q = 1u << 20;
constexpr u32 WS_P = 4u << 20;
constexpr u32 WS_T = 12u << 20;
constexpr u32 WS_DBG = 20u << 20;
constexpr u32 WS_END = 21u << 20;
constexpr u32 SLOT = 32768;
constexpr u32 SPIN_CAP = 1u << 18;
constexpr u32 CODE_LINES = 128;

DI u32 cvtpk(float lo, float hi) { f32x2 v = {lo, hi}; return __builtin_bit_cast(u32, __builtin_convertvector(v, bf16x2)); }
DI float max2f(float a, float b) { return __builtin_elementwise_maximum(a, b); }
DI float max3f(float a, float b, float c) { return max2f(max2f(a, b), c); }
DI float bflo(u32 x) { return __uint_as_float(x << 16); }
DI float bfhi(u32 x) { return __uint_as_float(x & 0xffff0000u); }
template <int N> DI void waitvm() { asm volatile("s_waitcnt vmcnt(%0)" ::"i"(N) : "memory"); }
DI void bar() { asm volatile("s_waitcnt lgkmcnt(0)\n\ts_barrier" ::: "memory"); }
template <int N> DI void barvm() { asm volatile("s_waitcnt vmcnt(%0) lgkmcnt(0)\n\ts_barrier" ::"i"(N) : "memory"); }
template <int IMM, int NT> DI void dma(u32 m0v, u32 voff, rsrc_t rs, u32 soff) {
  if constexpr (NT) asm volatile("s_mov_b32 m0, %0\n\ts_nop 0\n\tbuffer_load_dwordx4 %1, %2, %3 offen offset:%4 nt lds" ::"s"(m0v), "v"(voff), "s"(rs), "s"(soff), "i"(IMM) : "memory");
  else asm volatile("s_mov_b32 m0, %0\n\ts_nop 0\n\tbuffer_load_dwordx4 %1, %2, %3 offen offset:%4 lds" ::"s"(m0v), "v"(voff), "s"(rs), "s"(soff), "i"(IMM) : "memory");
}
template <int IMM> DI void ld16(u32x4& d, u32 voff, rsrc_t rs, u32 soff) {
  asm volatile("buffer_load_dwordx4 %0, %1, %2, %3 offen offset:%4 sc1" : "=v"(d) : "v"(voff), "s"(rs), "s"(soff), "i"(IMM) : "memory");
}
template <int IMM> DI void ld4(u32& d, u32 voff, rsrc_t rs, u32 soff) {
  asm volatile("buffer_load_dword %0, %1, %2, %3 offen offset:%4 sc1" : "=v"(d) : "v"(voff), "s"(rs), "s"(soff), "i"(IMM) : "memory");
}
template <int IMM> DI void st16(u32x4 v, u32 voff, rsrc_t rs, u32 soff) {
  asm volatile("buffer_store_dwordx4 %0, %1, %2, %3 offen offset:%4 sc1\n\ts_nop 1" ::"v"(v), "v"(voff), "s"(rs), "s"(soff), "i"(IMM) : "memory");
}
DI void st4(u32 v, u32 voff, rsrc_t rs, u32 soff) { __builtin_amdgcn_raw_buffer_store_b32(v, rs, voff, soff, 16); }
DI void poll(u32 voff, rsrc_t rs, u32 soff) {
  for (u32 n = 0; n < SPIN_CAP; ++n) {
    u32 v;
    asm volatile("buffer_load_dword %0, %1, %2, %3 offen sc1\n\ts_waitcnt vmcnt(0)" : "=&v"(v) : "v"(voff), "s"(rs), "s"(soff) : "memory");
    if (__all(v == 1u)) break;
  }
}
DI bf16x8 ldsb128(u32 a) { return *(const LAS bf16x8*)(size_t)a; }
DI bf16x8 ldstr(u32 a0, u32 a1) {
  bf16x4 lo = __builtin_amdgcn_ds_read_tr16_b64_v4bf16((LAS bf16x4*)(size_t)a0);
  bf16x4 hi = __builtin_amdgcn_ds_read_tr16_b64_v4bf16((LAS bf16x4*)(size_t)a1);
  return __builtin_shufflevector(lo, hi, 0, 1, 2, 3, 4, 5, 6, 7);
}
DI rsrc_t mkrs(const void* p, u32 bytes) { return __builtin_amdgcn_make_buffer_rsrc((void*)p, 0, bytes, 0x00020000); }

#define STAMP(i) do { u64 t_; asm volatile("s_memrealtime %0\n\ts_waitcnt lgkmcnt(0)" : "=s"(t_)::"memory"); stamp[i] = (u32)t_; } while (0)

__global__ __launch_bounds__(512, 2) void fused_kernel(const void* __restrict__ px, const void* __restrict__ pk, const void* __restrict__ pv,
                                                       const void* __restrict__ pwq, const void* __restrict__ pwo, void* __restrict__ pout,
                                                       void* __restrict__ pws) {
  extern __shared__ __attribute__((aligned(16))) char lds_raw[];
  u32 stamp[20], cyc0 = 0;
  for (int i = 0; i < 20; ++i) stamp[i] = 0;
  STAMP(0);
  const u32 tid = threadIdx.x, lane = tid & 63;
  u32 w = __builtin_amdgcn_readfirstlane(tid >> 6);
  asm volatile("s_nop 4" : "+s"(w));
  const u32 bid = blockIdx.x, xcd = bid & 7, jj = bid >> 3;
  const u32 kvh = xcd & 3, b = (xcd >> 2) * 4 + (jj >> 3), s = jj & 3, quad = kvh * 2 + ((jj >> 2) & 1);
  const u32 bp = b >> 1, par = b & 1, e = par * 4 + s, L = (b * 8 + quad) * 4 + s;
  const u32 wl = w & 3;
  const bool isB = w >= 4;
  const u32 lb = (u32)(size_t)(LAS char*)lds_raw;

  u32 pf;
  {
    u64 pc;
    asm volatile("s_getpc_b64 %0" : "=s"(pc));
    const u32 ln = (jj & 3) * 64 + w * 8 + (lane & 7);
    const char* cp = (const char*)(pc & ~127ull) + (ln < CODE_LINES ? ln : CODE_LINES - 1) * 128u;
    asm volatile("global_load_dword %0, %1, off" : "=v"(pf) : "v"(cp) : "memory");
  }
  const rsrc_t rx = mkrs(px, 256u * 1024 * 2), rk = mkrs(pk, 8u * 4 * 8192 * 256), rv = mkrs(pv, 8u * 4 * 8192 * 256);
  const rsrc_t rq = mkrs(pwq, 1024u * 4096 * 2), ro = mkrs(pwo, 4096u * 1024 * 2), rw = mkrs(pws, WS_END), rout = mkrs(pout, 256u * 1024 * 2);
  if (w == 0) { u32x2 z = {0u, 0u}; __builtin_amdgcn_raw_buffer_store_b64(z, rw, lane * 8, WS_FLAG + L * 512, 16); }

  const u32 l15 = lane & 15, g4 = lane >> 4, r32 = lane & 31, h2 = lane >> 5, q4 = l15 >> 2, p4 = lane & 3;

  const u32 kvbase = ((b * 4 + kvh) * 8192 + 6144 + s * 512) * 256 + wl * 8192;
  const u32 vK = g4 * 256 + ((l15 ^ g4) << 4);
  const u32 vV = g4 * 256 + ((l15 ^ (g4 << 2)) << 4);
  u32x4 qfrag;
  {
    const u32 xr0 = 8 * w + g4, xr1 = xr0 + 4;
    const u32 vX0 = xr0 * 2048 + ((l15 ^ (xr0 & 15)) << 4), vX1 = xr1 * 2048 + ((l15 ^ (xr1 & 15)) << 4) - 1024;
    const u32 wr = 16 * w + (lane >> 3), f0 = (wr >> 1) & 1;
    const u32 vW0 = wr * 8192 + (((lane & 7) ^ (f0 << 1)) << 4), vW1 = (wr + 8) * 8192 + (((lane & 7) ^ ((f0 | 2) << 1)) << 4) - 1024;
    const u32 sX = bp * 64 * 2048, sW = (quad * 512 + e * 64) * 2;
    const u32 mX = lb + w * 2048, mW = lb + 16384 + w * 2048;
#define QCHUNK(c_, slot_) do { dma<0, 0>(mX + (slot_), vX0, rx, sX + (c_) * 256); dma<1024, 0>(mX + (slot_), vX1, rx, sX + (c_) * 256); \
      dma<0, 0>(mW + (slot_), vW0, rq, sW + (c_) * (128 * 8192)); dma<1024, 0>(mW + (slot_), vW1, rq, sW + (c_) * (128 * 8192)); } while (0)
    QCHUNK(0, 2 * SLOT);
    QCHUNK(1, 3 * SLOT);
    const u32 dt = wl, rt = w >> 2;
    const u32 aA = lb + 16384 + (8 * g4 + q4) * 128 + ((dt ^ (((q4 >> 1) & 1) | ((g4 & 1) << 1))) << 5) + p4 * 8;
    const u32 aB = lb + (rt * 32 + l15) * 256 + ((g4 ^ l15) << 4);
    f32x4 acc0 = {0.f, 0.f, 0.f, 0.f}, acc1 = {0.f, 0.f, 0.f, 0.f};
    u32 rs_ = 2 * SLOT, ws_ = 4 * SLOT;
    asm volatile("s_waitcnt vmcnt(4)" : "+v"(pf)::"memory");
#pragma unroll 1
    for (u32 c = 0; c < 8; ++c) {
      if (c < 7) barvm<4>();
      else if (isB) barvm<8>();
      else barvm<0>();
      if (c < 6) QCHUNK(c + 2, ws_);
      else if (c == 6 && isB) {
        const u32 m = lb + wl * 8192;
        dma<0, KV_NT>(m, vK, rk, kvbase); dma<1024, KV_NT>(m, vK ^ 64, rk, kvbase); dma<2048, KV_NT>(m, vK ^ 128, rk, kvbase); dma<3072, KV_NT>(m, vK ^ 192, rk, kvbase);
        dma<0, KV_NT>(m + 4096, vK, rk, kvbase + 4096); dma<1024, KV_NT>(m + 4096, vK ^ 64, rk, kvbase + 4096);
        dma<2048, KV_NT>(m + 4096, vK ^ 128, rk, kvbase + 4096); dma<3072, KV_NT>(m + 4096, vK ^ 192, rk, kvbase + 4096);
      }
      const u32 a = aA + rs_;
#pragma unroll
      for (int ks = 0; ks < 4; ++ks) {
        const bf16x8 fa = ldstr(a + ks * 4096, a + ks * 4096 + 512);
        const u32 bb = (aB ^ (ks * 64)) + rs_;
        const bf16x8 f0_ = ldsb128(bb), f1_ = ldsb128(bb + 4096);
        acc0 = __builtin_amdgcn_mfma_f32_16x16x32_bf16(fa, f0_, acc0, 0, 0, 0);
        acc1 = __builtin_amdgcn_mfma_f32_16x16x32_bf16(fa, f1_, acc1, 0, 0, 0);
      }
      rs_ = rs_ == 4 * SLOT ? 0 : rs_ + SLOT;
      ws_ = ws_ == 4 * SLOT ? 0 : ws_ + SLOT;
    }
#undef QCHUNK
    const float cs = 0.12751743082459868f;
    const auto s0 = __builtin_amdgcn_permlane16_swap(cvtpk(acc0[0] * cs, acc0[1] * cs), cvtpk(acc1[0] * cs, acc1[1] * cs), false, false);
    const auto s1 = __builtin_amdgcn_permlane16_swap(cvtpk(acc0[2] * cs, acc0[3] * cs), cvtpk(acc1[2] * cs, acc1[3] * cs), false, false);
    qfrag = u32x4{s0[0], s1[0], s0[1], s1[1]};
  }
  STAMP(1);
  const u32 cflag_v = lane < 4 ? lane * 512 : 0x7fffffffu;
  const u32 cflag_s = WS_FLAG + (((bp * 2 + (w >> 2)) * 8 + quad) * 4) * 512 + (e * 4 + wl) * 4;
  st16<0>(qfrag, lane * 16, rw, WS_Q + ((((bp * 2 + (w >> 2)) * 32 + quad * 4) * 8 + e * 4 + wl) << 10));
  bf16x8 qf[8];
  if (isB) {
#pragma unroll 1
    for (u32 hc = 2; hc < 8; ++hc) {
      const u32 off = (hc >> 1) * SLOT + (hc & 1) * 4096, m = lb + wl * 8192 + off;
      waitvm<7>(); dma<0, KV_NT>(m, vK, rk, kvbase + off);
      waitvm<7>(); dma<1024, KV_NT>(m, vK ^ 64, rk, kvbase + off);
      waitvm<7>(); dma<2048, KV_NT>(m, vK ^ 128, rk, kvbase + off);
      waitvm<7>();
      if (hc == 3) st4(1u, cflag_v, rw, cflag_s);
      dma<3072, KV_NT>(m, vK ^ 192, rk, kvbase + off);
      if (hc == 5) barvm<8>();
    }
#pragma unroll 1
    for (u32 hc = 0; hc < 2; ++hc) {
      const u32 m = lb + 4 * SLOT + wl * 8192 + hc * 4096, so = kvbase + hc * 4096;
      waitvm<7>(); dma<0, KV_NT>(m, vV, rv, so);
      waitvm<7>(); dma<1024, KV_NT>(m, vV, rv, so);
      waitvm<7>(); dma<2048, KV_NT>(m, vV, rv, so);
      waitvm<7>(); dma<3072, KV_NT>(m, vV, rv, so);
    }
    STAMP(2);
    bar();
    barvm<8>();
#pragma unroll
    for (int d = 0; d < 8; ++d) qf[d] = ldsb128(lb + wl * 8192 + d * 1024 + lane * 16);
  } else {
    waitvm<0>();
    st4(1u, cflag_v, rw, cflag_s);
    bar();
    poll((wl * 8 + (lane & 7)) * 4, rw, WS_FLAG + L * 512);
    STAMP(2);
    u32x4 t[8];
    const u32 so = WS_Q + (((b * 32 + quad * 4 + wl) * 8) << 10);
    ld16<0>(t[0], lane * 16, rw, so); ld16<1024>(t[1], lane * 16, rw, so); ld16<2048>(t[2], lane * 16, rw, so); ld16<3072>(t[3], lane * 16, rw, so);
    ld16<0>(t[4], lane * 16, rw, so + 4096); ld16<1024>(t[5], lane * 16, rw, so + 4096); ld16<2048>(t[6], lane * 16, rw, so + 4096); ld16<3072>(t[7], lane * 16, rw, so + 4096);
    asm volatile("s_waitcnt vmcnt(0)" : "+v"(t[0]), "+v"(t[1]), "+v"(t[2]), "+v"(t[3]), "+v"(t[4]), "+v"(t[5]), "+v"(t[6]), "+v"(t[7])::"memory");
#pragma unroll
    for (int d = 0; d < 8; ++d) qf[d] = __builtin_bit_cast(bf16x8, t[d]);
  }
  STAMP(3);
  f32x16 S[8];
  float mx;
  {
    const u32 aK = lb + (isB ? 2 * SLOT : 0) + r32 * 256 + ((h2 ^ l15) << 4);
    float m0 = -__builtin_inff(), m1 = -__builtin_inff();
    constexpr int PD = 6;
    bf16x8 kf[PD];
#pragma unroll
    for (int n = 0; n < PD; ++n) kf[n] = ldsb128((aK ^ ((n & 7) * 32)) + (n >> 3) * 8192);
    PIN();
#pragma unroll
    for (int n = 0; n < 64; ++n) {
      const int t = n >> 3, d = n & 7;
      if (d == 0) S[t] = f32x16{};
      S[t] = __builtin_amdgcn_mfma_f32_32x32x16_bf16(kf[n % PD], qf[d], S[t], 0, 0, 0);
      if (n + PD < 64) kf[n % PD] = ldsb128((aK ^ (((n + PD) & 7) * 32)) + ((n + PD) >> 3) * 8192);
      if (t > 0 && d >= 4) { const int i = (d - 4) * 4; m0 = max3f(m0, S[t - 1][i], S[t - 1][i + 1]); m1 = max3f(m1, S[t - 1][i + 2], S[t - 1][i + 3]); asm volatile("" : "+v"(m0), "+v"(m1)); }
      PIN();
      if (n == 31) {
        asm volatile("s_barrier" ::: "memory");
        if (!isB) {
#pragma unroll
          for (int dd = 0; dd < 8; ++dd) *(LAS bf16x8*)(size_t)(lb + wl * 8192 + dd * 1024 + lane * 16) = qf[dd];
        }
        PIN();
      }
    }
    barvm<0>();
    PIN();
#pragma unroll
    for (int i = 0; i < 16; i += 4) { m0 = max3f(m0, S[7][i], S[7][i + 1]); m1 = max3f(m1, S[7][i + 2], S[7][i + 3]); }
    mx = max2f(m0, m1);
    const auto sw = __builtin_amdgcn_permlane32_swap(__float_as_uint(mx), __float_as_uint(mx), false, false);
    mx = max2f(__uint_as_float(sw[0]), __uint_as_float(sw[1]));
  }
  STAMP(4);
  st4(1u, cflag_v, rw, cflag_s);
  u32x4 P[16];
  float ls, mA = 0.f, lA = 0.f;
  {
    float l0 = 0.f, l1 = 0.f, l2 = 0.f, l3 = 0.f;
#pragma unroll
    for (int hf = 0; hf < 2; ++hf) {
      const u32 ch = isB ? 3 : 1 + hf, m = lb + (isB ? 2 : 1 - hf) * SLOT + wl * 8192, so = kvbase + ch * SLOT;
      const bool en = !(isB && hf == 1);
      if (hf == 1 && isB) { mA = *(const LAS float*)(size_t)(lb + 3 * SLOT + wl * 256 + r32 * 4); lA = *(const LAS float*)(size_t)(lb + 3 * SLOT + wl * 256 + 128 + r32 * 4); }
#pragma unroll
      for (int u = 0; u < 8; ++u) {
        if (en) {
          if ((u & 3) == 0) dma<0, KV_NT>(m + (u >> 2) * 4096, vV, rv, so + (u >> 2) * 4096);
          if ((u & 3) == 1) dma<1024, KV_NT>(m + (u >> 2) * 4096, vV, rv, so + (u >> 2) * 4096);
          if ((u & 3) == 2) dma<2048, KV_NT>(m + (u >> 2) * 4096, vV, rv, so + (u >> 2) * 4096);
          if ((u & 3) == 3) dma<3072, KV_NT>(m + (u >> 2) * 4096, vV, rv, so + (u >> 2) * 4096);
        }
        PIN();
        const int t = hf * 4 + (u >> 1), o = (u & 1) * 8;
        float p[8];
#pragma unroll
        for (int i = 0; i < 8; ++i) p[i] = __builtin_amdgcn_exp2f(S[t][o + i] - mx);
        l0 += p[0]; l1 += p[1]; l2 += p[2]; l3 += p[3]; l0 += p[4]; l1 += p[5]; l2 += p[6]; l3 += p[7];
        P[hf * 8 + u] = u32x4{cvtpk(p[0], p[1]), cvtpk(p[2], p[3]), cvtpk(p[4], p[5]), cvtpk(p[6], p[7])};
        asm volatile("" : "+v"(P[hf * 8 + u]), "+v"(l0), "+v"(l1), "+v"(l2), "+v"(l3));
        PIN();
      }
      bar();
    }
    ls = (l0 + l1) + (l2 + l3);
    const auto sw = __builtin_amdgcn_permlane32_swap(__float_as_uint(ls), __float_as_uint(ls), false, false);
    ls = __uint_as_float(sw[0]) + __uint_as_float(sw[1]);
  }
  STAMP(5);
  if (!isB) *(LAS float*)(size_t)(lb + 3 * SLOT + wl * 256 + lane * 4) = h2 ? ls : mx;
  f32x16 O[4] = {};
  f32x16 Dm[4];
  float fB = 0.f, stat = 0.f;
  {
    const u32 aV = lb + (4 * h2 + q4) * 256 + (q4 << 6) + (g4 & 1) * 32 + p4 * 8;
#pragma unroll
    for (int hf = 0; hf < 2; ++hf) {
      const u32 vb = (isB ? 2 * hf : 4 - 3 * hf) * SLOT;
      constexpr int PD = 6;
      bf16x8 vf[PD];
#pragma unroll
      for (int n = 0; n < PD; ++n) { const u32 a = (aV ^ ((n & 3) << 6)) + vb + (n >> 2) * 4096; vf[n] = ldstr(a, a + 2048); }
      PIN();
#pragma unroll
      for (int n = 0; n < 32; ++n) {
        O[n & 3] = __builtin_amdgcn_mfma_f32_32x32x16_bf16(vf[n % PD], __builtin_bit_cast(bf16x8, P[hf * 8 + (n >> 2)]), O[n & 3], 0, 0, 0);
        if (n + PD < 32) { const u32 a = (aV ^ (((n + PD) & 3) << 6)) + vb + ((n + PD) >> 2) * 4096; vf[n % PD] = ldstr(a, a + 2048); }
        PIN();
      }
      if (hf == 0) {
        barvm<8>();
        STAMP(6);
        if (isB) {
          const float M = max2f(mA, mx), fa = __builtin_amdgcn_exp2f(mA - M), fb = __builtin_amdgcn_exp2f(mx - M);
          const float Ls = fa * lA + fb * ls, inv = 1.0f / Ls, fA = fa * inv;
          fB = fb * inv;
          stat = M + __builtin_amdgcn_logf(Ls);
          const u32 da = lb + (wl < 2 ? 4 : 1) * SLOT + (wl & 1) * 16384 + lane * 16;
#pragma unroll
          for (int dt = 0; dt < 4; ++dt)
#pragma unroll
            for (int i = 0; i < 4; ++i) {
              const f32x4 v = *(const LAS f32x4*)(size_t)(da + (dt * 4 + i) * 1024);
              Dm[dt][4 * i] = v[0] * fA; Dm[dt][4 * i + 1] = v[1] * fA; Dm[dt][4 * i + 2] = v[2] * fA; Dm[dt][4 * i + 3] = v[3] * fA;
            }
          asm volatile("" : "+v"(Dm[0]), "+v"(Dm[1]), "+v"(Dm[2]), "+v"(Dm[3]));
          barvm<0>();
          STAMP(7);
        }
      }
    }
  }
  STAMP(8);
  if (!isB) {
    barvm<0>();
    const u32 da = lb + (wl < 2 ? 4 : 1) * SLOT + (wl & 1) * 16384 + lane * 16;
#pragma unroll
    for (int dt = 0; dt < 4; ++dt)
#pragma unroll
      for (int i = 0; i < 4; ++i) *(LAS f32x4*)(size_t)(da + (dt * 4 + i) * 1024) = f32x4{O[dt][4 * i], O[dt][4 * i + 1], O[dt][4 * i + 2], O[dt][4 * i + 3]};
    bar();
    const u32 vW0 = h2 * 2048 + ((r32 ^ (h2 << 2)) << 4), vW1 = h2 * 2048 + 3072 + ((r32 ^ ((2 + h2) << 2)) << 4);
    u32 so = ((quad * 4 + par * 2) * 128 + 16 * wl) * 2048 + s * 512;
#pragma unroll 1
    for (u32 c = 0; c < 4; ++c) {
      const u32 m = lb + ((0x1403u >> (4 * c)) & 7) * SLOT + wl * 8192;
#pragma unroll
      for (int i = 0; i < 4; ++i) {
        waitvm<7>(); dma<0, 0>(m + i * 2048, vW0, ro, so + i * 8192);
        waitvm<7>(); dma<1024, 0>(m + i * 2048, vW1, ro, so + i * 8192);
      }
      so += 64 * 2048;
      if (c == 0) bar();
    }
    STAMP(9);
    barvm<0>();
  } else {
    const u32 hs = (b * 32 + quad * 4 + wl) * 4 + s;
#pragma unroll
    for (int dt = 0; dt < 4; ++dt)
#pragma unroll
      for (int u = 0; u < 2; ++u) {
        float o[8];
#pragma unroll
        for (int i = 0; i < 8; ++i) o[i] = fmaf(O[dt][8 * u + i], fB, Dm[dt][8 * u + i]);
        const u32x4 ov = {cvtpk(o[0], o[1]), cvtpk(o[2], o[3]), cvtpk(o[4], o[5]), cvtpk(o[6], o[7])};
        if (u == 0) st16<0>(ov, lane * 16, rw, WS_P + hs * 8192 + dt * 2048); else st16<1024>(ov, lane * 16, rw, WS_P + hs * 8192 + dt * 2048);
      }
    st4(__float_as_uint(stat), lane < 32 ? lane * 4 : 0x7fffffffu, rw, WS_STAT + hs * 128);
    waitvm<0>();
    st4(1u, cflag_v, rw, WS_FLAG + (((bp * 2 + (wl >> 1)) * 8 + quad) * 4) * 512 + 128 + ((par * 2 + (wl & 1)) * 4 + s) * 4);
    STAMP(9);
    bar();
  }
  STAMP(10);
  f32x16 acc[2] = {};
  {
    poll(((w >> 1) * 4 + p4) * 4, rw, WS_FLAG + L * 512 + 128);
    STAMP(11);
    const u32 hs = ((bp * 2 + (w >> 2)) * 32 + quad * 4 + par * 2 + ((w >> 1) & 1)) * 4;
    u32 st[4];
    u32x4 G[4][4];
    ld4<0>(st[0], r32 * 4, rw, WS_STAT + hs * 128); ld4<128>(st[1], r32 * 4, rw, WS_STAT + hs * 128);
    ld4<256>(st[2], r32 * 4, rw, WS_STAT + hs * 128); ld4<384>(st[3], r32 * 4, rw, WS_STAT + hs * 128);
    const u32 so = WS_P + hs * 8192 + (w & 1) * 4096;
#pragma unroll
    for (int sp = 0; sp < 4; ++sp) ld16<0>(G[0][sp], lane * 16, rw, so + sp * 8192);
#pragma unroll
    for (int sp = 0; sp < 4; ++sp) ld16<1024>(G[1][sp], lane * 16, rw, so + sp * 8192);
#pragma unroll
    for (int sp = 0; sp < 4; ++sp) ld16<2048>(G[2][sp], lane * 16, rw, so + sp * 8192);
#pragma unroll
    for (int sp = 0; sp < 4; ++sp) ld16<3072>(G[3][sp], lane * 16, rw, so + sp * 8192);
    bf16x8 Wf[4][4];
    const u32 p4s = ((p4 & 1) << 1) | (p4 >> 1);
    const u32 aW = lb + (4 * h2 + q4) * 512 + ((w ^ q4) << 6) + (g4 & 1) * 32 + p4s * 8;
#pragma unroll
    for (int c = 0; c < 4; ++c)
#pragma unroll
      for (int i = 0; i < 4; ++i) {
        const u32 a = aW + ((0x1403u >> (4 * c)) & 7) * SLOT + i * 8192;
        Wf[c][i] = ldstr(a, a + 4096);
      }
    asm volatile("s_waitcnt vmcnt(16)" : "+v"(st[0]), "+v"(st[1]), "+v"(st[2]), "+v"(st[3])::"memory");
    float wt[4];
    {
      const float s0 = __uint_as_float(st[0]), s1 = __uint_as_float(st[1]), s2 = __uint_as_float(st[2]), s3 = __uint_as_float(st[3]);
      const float M = max2f(max2f(s0, s1), max2f(s2, s3));
      wt[0] = __builtin_amdgcn_exp2f(s0 - M); wt[1] = __builtin_amdgcn_exp2f(s1 - M); wt[2] = __builtin_amdgcn_exp2f(s2 - M); wt[3] = __builtin_amdgcn_exp2f(s3 - M);
      const float inv = 1.0f / ((wt[0] + wt[1]) + (wt[2] + wt[3]));
      wt[0] *= inv; wt[1] *= inv; wt[2] *= inv; wt[3] *= inv;
    }
#pragma unroll
    for (int i = 0; i < 4; ++i) {
      if (i == 0) asm volatile("s_waitcnt vmcnt(12)" : "+v"(G[0][0]), "+v"(G[0][1]), "+v"(G[0][2]), "+v"(G[0][3])::"memory");
      if (i == 1) asm volatile("s_waitcnt vmcnt(8)" : "+v"(G[1][0]), "+v"(G[1][1]), "+v"(G[1][2]), "+v"(G[1][3])::"memory");
      if (i == 2) asm volatile("s_waitcnt vmcnt(4)" : "+v"(G[2][0]), "+v"(G[2][1]), "+v"(G[2][2]), "+v"(G[2][3])::"memory");
      if (i == 3) asm volatile("s_waitcnt vmcnt(0)" : "+v"(G[3][0]), "+v"(G[3][1]), "+v"(G[3][2]), "+v"(G[3][3])::"memory");
      u32x4 o;
#pragma unroll
      for (int d = 0; d < 4; ++d) {
        float lo = bflo(G[i][0][d]) * wt[0], hi = bfhi(G[i][0][d]) * wt[0];
#pragma unroll
        for (int sp = 1; sp < 4; ++sp) { lo = fmaf(bflo(G[i][sp][d]), wt[sp], lo); hi = fmaf(bfhi(G[i][sp][d]), wt[sp], hi); }
        o[d] = cvtpk(lo, hi);
      }
      *(LAS u32x4*)(size_t)(lb + 2 * SLOT + (i * 8 + w) * 1024 + lane * 16) = o;
      bar();
#pragma unroll
      for (int c = 0; c < 4; ++c)
#pragma unroll
        for (int rb = 0; rb < 2; ++rb)
          acc[rb] = __builtin_amdgcn_mfma_f32_32x32x16_bf16(Wf[c][i], ldsb128(lb + 2 * SLOT + (i * 8 + rb * 4 + c) * 1024 + lane * 16), acc[rb], 0, 0, 0);
    }
  }
  STAMP(12);
  {
    const u32 src = quad * 2 + par, Ld = ((bp * 2) * 8 + w) * 4 + s;
#pragma unroll
    for (int rb = 0; rb < 2; ++rb)
#pragma unroll
      for (int t = 0; t < 2; ++t) {
        const u32x4 ov = {cvtpk(acc[rb][8 * t], acc[rb][8 * t + 1]), cvtpk(acc[rb][8 * t + 2], acc[rb][8 * t + 3]),
                          cvtpk(acc[rb][8 * t + 4], acc[rb][8 * t + 5]), cvtpk(acc[rb][8 * t + 6], acc[rb][8 * t + 7])};
        if (t == 0) st16<0>(ov, (r32 * 2 + h2) * 16, rw, WS_T + ((Ld + 32 * rb) * 16 + src) * 2048);
        else st16<1024>(ov, (r32 * 2 + h2) * 16, rw, WS_T + ((Ld + 32 * rb) * 16 + src) * 2048);
      }
    waitvm<0>();
    const u32 gflag_v = lane < 2 ? lane * (32 * 512) : 0x7fffffffu, gflag_s = WS_FLAG + Ld * 512 + 256 + src * 4;
    st4(1u, gflag_v, rw, gflag_s);
    STAMP(13);
    poll(l15 * 4, rw, WS_FLAG + L * 512 + 256);
    STAMP(14);
    u32x4 T[4];
    const u32 vo = g4 * 2048 + w * 256 + l15 * 16, so = WS_T + L * 32768;
#pragma unroll
    for (int i = 0; i < 4; ++i) ld16<0>(T[i], vo, rw, so + i * 8192);
    st4(1u, gflag_v, rw, gflag_s);
    asm volatile("s_waitcnt vmcnt(1)" : "+v"(T[0]), "+v"(T[1]), "+v"(T[2]), "+v"(T[3])::"memory");
    float v[8];
#pragma unroll
    for (int d = 0; d < 4; ++d) {
      v[2 * d] = (bflo(T[0][d]) + bflo(T[1][d])) + (bflo(T[2][d]) + bflo(T[3][d]));
      v[2 * d + 1] = (bfhi(T[0][d]) + bfhi(T[1][d])) + (bfhi(T[2][d]) + bfhi(T[3][d]));
    }
    float u[4], z[2];
#pragma unroll
    for (int i = 0; i < 4; ++i) {
      const auto sw = __builtin_amdgcn_permlane32_swap(__float_as_uint(v[i]), __float_as_uint(v[i + 4]), false, false);
      u[i] = __uint_as_float(sw[0]) + __uint_as_float(sw[1]);
    }
#pragma unroll
    for (int i = 0; i < 2; ++i) {
      const auto sw = __builtin_amdgcn_permlane16_swap(__float_as_uint(u[i]), __float_as_uint(u[i + 2]), false, false);
      z[i] = __uint_as_float(sw[0]) + __uint_as_float(sw[1]);
    }
    __builtin_amdgcn_raw_buffer_store_b32(cvtpk(z[0], z[1]), rout, (l15 >> 1) * 2048 + (l15 & 1) * 16 + g4 * 4,
                                          (b * 32 + wl * 8) * 2048 + (s * 256 + quad * 32 + (w >> 2) * 16) * 2, 0);
  }
  waitvm<0>();
  STAMP(15);
  if (lane == 0) for (int i = 0; i < 16; ++i) ((u32*)((char*)pws + WS_DBG))[(bid * 8 + w) * 16 + i] = stamp[i];
}

__global__ void spin_kernel(const u32* st){
  __shared__ u32 mn[256], mxx[256]; __shared__ float sm[256];
  u32 ref=st[0]-100000u;
  u32 a=~0u,b=0; float acc=0.f;
  for(int i=threadIdx.x;i<2048;i+=256){ u32 x=st[i*16+0]-ref, y=st[i*16+15]-ref; a=min(a,x); b=max(b,y); acc+=(float)y; }
  mn[threadIdx.x]=a; mxx[threadIdx.x]=b; sm[threadIdx.x]=acc; __syncthreads();
  for(int o=128;o>0;o>>=1){ if(threadIdx.x<o){ mn[threadIdx.x]=min(mn[threadIdx.x],mn[threadIdx.x+o]); mxx[threadIdx.x]=max(mxx[threadIdx.x],mxx[threadIdx.x+o]); sm[threadIdx.x]+=sm[threadIdx.x+o]; } __syncthreads(); }
  if(threadIdx.x==0){ float e = 1 ? (float)mxx[0] : sm[0]/2048.f; u64 n=(u64)((e-(float)mn[0])*0.f); if(n>1000000) n=1000000; u64 t0=wall_clock64(); while(wall_clock64()-t0<n){} }
}
extern "C" void kernel_launch(void* const* d_in, const int* in_sizes, int n_in, void* d_out, int out_size, void* d_ws, size_t ws_size,
                              hipStream_t stream) {
  (void)hipFuncSetAttribute((const void*)fused_kernel, hipFuncAttributeMaxDynamicSharedMemorySize, 163840);
  fused_kernel<<<256, 512, 163840, stream>>>(d_in[0], d_in[1], d_in[2], d_in[3], d_in[4], d_out, d_ws);
  spin_kernel<<<1,256,0,stream>>>((const u32*)((char*)d_ws+WS_DBG));
}
```

```cpp
#include <hip/hip_runtime.h>

typedef unsigned u32;
typedef unsigned long long u64;
typedef u32 u32x2 __attribute__((ext_vector_type(2)));
typedef u32 u32x4 __attribute__((ext_vector_type(4)));
typedef float f32x2 __attribute__((ext_vector_type(2)));
typedef float f32x4 __attribute__((ext_vector_type(4)));
typedef float f32x16 __attribute__((ext_vector_type(16)));
typedef __bf16 bf16x2 __attribute__((ext_vector_type(2)));
typedef __bf16 bf16x4 __attribute__((ext_vector_type(4)));
typedef __bf16 bf16x8 __attribute__((ext_vector_type(8)));
typedef __amdgpu_buffer_rsrc_t rsrc_t;
#define LAS __attribute__((address_space(3)))
#define DI __device__ __forceinline__
#define PIN() __builtin_amdgcn_sched_barrier(0)

#define KV_NT 0
#define STAMPS 0

constexpr u32 WS_FLAG = 0;
constexpr u32 WS_STAT = 1u << 17;
constexpr u32 WS_Q = 1u << 20;
constexpr u32 WS_P = 4u << 20;
constexpr u32 WS_T = 12u << 20;
constexpr u32 WS_DBG = 20u << 20;
constexpr u32 WS_END = 21u << 20;
constexpr u32 SLOT = 32768;
constexpr u32 SPIN_CAP = 1u << 18;
constexpr u32 CODE_LINES = 128;

DI u32 cvtpk(float lo, float hi) { f32x2 v = {lo, hi}; return __builtin_bit_cast(u32, __builtin_convertvector(v, bf16x2)); }
DI float max2f(float a, float b) { return __builtin_elementwise_maximum(a, b); }
DI float max3f(float a, float b, float c) { return max2f(max2f(a, b), c); }
DI float bflo(u32 x) { return __uint_as_float(x << 16); }
DI float bfhi(u32 x) { return __uint_as_float(x & 0xffff0000u); }
template <int N> DI void waitvm() { asm volatile("s_waitcnt vmcnt(%0)" ::"i"(N) : "memory"); }
DI void bar() { asm volatile("s_waitcnt lgkmcnt(0)\n\ts_barrier" ::: "memory"); }
template <int N> DI void barvm() { asm volatile("s_waitcnt vmcnt(%0) lgkmcnt(0)\n\ts_barrier" ::"i"(N) : "memory"); }
template <int IMM, int NT> DI void dma(u32 m0v, u32 voff, rsrc_t rs, u32 soff) {
  if constexpr (NT) asm volatile("s_mov_b32 m0, %0\n\ts_nop 0\n\tbuffer_load_dwordx4 %1, %2, %3 offen offset:%4 nt lds" ::"s"(m0v), "v"(voff), "s"(rs), "s"(soff), "i"(IMM) : "memory");
  else asm volatile("s_mov_b32 m0, %0\n\ts_nop 0\n\tbuffer_load_dwordx4 %1, %2, %3 offen offset:%4 lds" ::"s"(m0v), "v"(voff), "s"(rs), "s"(soff), "i"(IMM) : "memory");
}
template <int IMM> DI void ld16(u32x4& d, u32 voff, rsrc_t rs, u32 soff) {
  asm volatile("buffer_load_dwordx4 %0, %1, %2, %3 offen offset:%4 sc1" : "=v"(d) : "v"(voff), "s"(rs), "s"(soff), "i"(IMM) : "memory");
}
template <int IMM> DI void ld4(u32& d, u32 voff, rsrc_t rs, u32 soff) {
  asm volatile("buffer_load_dword %0, %1, %2, %3 offen offset:%4 sc1" : "=v"(d) : "v"(voff), "s"(rs), "s"(soff), "i"(IMM) : "memory");
}
template <int IMM> DI void st16(u32x4 v, u32 voff, rsrc_t rs, u32 soff) {
  asm volatile("buffer_store_dwordx4 %0, %1, %2, %3 offen offset:%4 sc1\n\ts_nop 1" ::"v"(v), "v"(voff), "s"(rs), "s"(soff), "i"(IMM) : "memory");
}
DI void st4(u32 v, u32 voff, rsrc_t rs, u32 soff) { __builtin_amdgcn_raw_buffer_store_b32(v, rs, voff, soff, 16); }
DI void poll(u32 voff, rsrc_t rs, u32 soff) {
  for (u32 n = 0; n < SPIN_CAP; ++n) {
    u32 v;
    asm volatile("buffer_load_dword %0, %1, %2, %3 offen sc1\n\ts_waitcnt vmcnt(0)" : "=&v"(v) : "v"(voff), "s"(rs), "s"(soff) : "memory");
    if (__all(v == 1u)) break;
  }
}
DI bf16x8 ldsb128(u32 a) { return *(const LAS bf16x8*)(size_t)a; }
DI bf16x8 ldstr(u32 a0, u32 a1) {
  bf16x4 lo = __builtin_amdgcn_ds_read_tr16_b64_v4bf16((LAS bf16x4*)(size_t)a0);
  bf16x4 hi = __builtin_amdgcn_ds_read_tr16_b64_v4bf16((LAS bf16x4*)(size_t)a1);
  return __builtin_shufflevector(lo, hi, 0, 1, 2, 3, 4, 5, 6, 7);
}
DI rsrc_t mkrs(const void* p, u32 bytes) { return __builtin_amdgcn_make_buffer_rsrc((void*)p, 0, bytes, 0x00020000); }

#define STAMP(i)

__global__ __launch_bounds__(512, 2) void fused_kernel(const void* __restrict__ px, const void* __restrict__ pk, const void* __restrict__ pv,
                                                       const void* __restrict__ pwq, const void* __restrict__ pwo, void* __restrict__ pout,
                                                       void* __restrict__ pws) {
  extern __shared__ __attribute__((aligned(16))) char lds_raw[];
  STAMP(0);
  const u32 tid = threadIdx.x, lane = tid & 63;
  u32 w = __builtin_amdgcn_readfirstlane(tid >> 6);
  asm volatile("s_nop 4" : "+s"(w));
  const u32 bid = blockIdx.x, xcd = bid & 7, jj = bid >> 3;
  const u32 kvh = xcd & 3, b = (xcd >> 2) * 4 + (jj >> 3), s = jj & 3, quad = kvh * 2 + ((jj >> 2) & 1);
  const u32 bp = b >> 1, par = b & 1, e = par * 4 + s, L = (b * 8 + quad) * 4 + s;
  const u32 wl = w & 3;
  const bool isB = w >= 4;
  const u32 lb = (u32)(size_t)(LAS char*)lds_raw;

  u32 pf;
  {
    u64 pc;
    asm volatile("s_getpc_b64 %0" : "=s"(pc));
    const u32 ln = (jj & 3) * 64 + w * 8 + (lane & 7);
    const char* cp = (const char*)(pc & ~127ull) + (ln < CODE_LINES ? ln : CODE_LINES - 1) * 128u;
    asm volatile("global_load_dword %0, %1, off" : "=v"(pf) : "v"(cp) : "memory");
  }
  const rsrc_t rx = mkrs(px, 256u * 1024 * 2), rk = mkrs(pk, 8u * 4 * 8192 * 256), rv = mkrs(pv, 8u * 4 * 8192 * 256);
  const rsrc_t rq = mkrs(pwq, 1024u * 4096 * 2), ro = mkrs(pwo, 4096u * 1024 * 2), rw = mkrs(pws, WS_END), rout = mkrs(pout, 256u * 1024 * 2);
  if (w == 0) { u32x2 z = {0u, 0u}; __builtin_amdgcn_raw_buffer_store_b64(z, rw, lane * 8, WS_FLAG + L * 512, 16); }

  const u32 l15 = lane & 15, g4 = lane >> 4, r32 = lane & 31, h2 = lane >> 5, q4 = l15 >> 2, p4 = lane & 3;

  const u32 kvbase = ((b * 4 + kvh) * 8192 + 6144 + s * 512) * 256 + wl * 8192;
  const u32 vK = g4 * 256 + ((l15 ^ g4) << 4);
  const u32 vV = g4 * 256 + ((l15 ^ (g4 << 2)) << 4);
  u32x4 qfrag;
  {
    const u32 xr0 = 8 * w + g4, xr1 = xr0 + 4;
    const u32 vX0 = xr0 * 2048 + ((l15 ^ (xr0 & 15)) << 4), vX1 = xr1 * 2048 + ((l15 ^ (xr1 & 15)) << 4) - 1024;
    const u32 wr = 16 * w + (lane >> 3), f0 = (wr >> 1) & 1;
    const u32 vW0 = wr * 8192 + (((lane & 7) ^ (f0 << 1)) << 4), vW1 = (wr + 8) * 8192 + (((lane & 7) ^ ((f0 | 2) << 1)) << 4) - 1024;
    const u32 sX = bp * 64 * 2048, sW = (quad * 512 + e * 64) * 2;
    const u32 mX = lb + w * 2048, mW = lb + 16384 + w * 2048;
#define QCHUNK(c_, slot_) do { dma<0, 0>(mX + (slot_), vX0, rx, sX + (c_) * 256); dma<1024, 0>(mX + (slot_), vX1, rx, sX + (c_) * 256); \
      dma<0, 0>(mW + (slot_), vW0, rq, sW + (c_) * (128 * 8192)); dma<1024, 0>(mW + (slot_), vW1, rq, sW + (c_) * (128 * 8192)); } while (0)
    QCHUNK(0, 2 * SLOT);
    QCHUNK(1, 3 * SLOT);
    const u32 dt = wl, rt = w >> 2;
    const u32 aA = lb + 16384 + (8 * g4 + q4) * 128 + ((dt ^ (((q4 >> 1) & 1) | ((g4 & 1) << 1))) << 5) + p4 * 8;
    const u32 aB = lb + (rt * 32 + l15) * 256 + ((g4 ^ l15) << 4);
    f32x4 acc0 = {0.f, 0.f, 0.f, 0.f}, acc1 = {0.f, 0.f, 0.f, 0.f};
    u32 rs_ = 2 * SLOT, ws_ = 4 * SLOT;
    asm volatile("s_waitcnt vmcnt(4)" : "+v"(pf)::"memory");
#pragma unroll 1
    for (u32 c = 0; c < 8; ++c) {
      if (c < 7) barvm<4>();
      else if (isB) barvm<8>();
      else barvm<0>();
      if (c < 6) QCHUNK(c + 2, ws_);
      else if (c == 6 && isB) {
        const u32 m = lb + wl * 8192;
        dma<0, KV_NT>(m, vK, rk, kvbase); dma<1024, KV_NT>(m, vK ^ 64, rk, kvbase); dma<2048, KV_NT>(m, vK ^ 128, rk, kvbase); dma<3072, KV_NT>(m, vK ^ 192, rk, kvbase);
        dma<0, KV_NT>(m + 4096, vK, rk, kvbase + 4096); dma<1024, KV_NT>(m + 4096, vK ^ 64, rk, kvbase + 4096);
        dma<2048, KV_NT>(m + 4096, vK ^ 128, rk, kvbase + 4096); dma<3072, KV_NT>(m + 4096, vK ^ 192, rk, kvbase + 4096);
      }
      const u32 a = aA + rs_;
#pragma unroll
      for (int ks = 0; ks < 4; ++ks) {
        const bf16x8 fa = ldstr(a + ks * 4096, a + ks * 4096 + 512);
        const u32 bb = (aB ^ (ks * 64)) + rs_;
        const bf16x8 f0_ = ldsb128(bb), f1_ = ldsb128(bb + 4096);
        acc0 = __builtin_amdgcn_mfma_f32_16x16x32_bf16(fa, f0_, acc0, 0, 0, 0);
        acc1 = __builtin_amdgcn_mfma_f32_16x16x32_bf16(fa, f1_, acc1, 0, 0, 0);
      }
      rs_ = rs_ == 4 * SLOT ? 0 : rs_ + SLOT;
      ws_ = ws_ == 4 * SLOT ? 0 : ws_ + SLOT;
    }
#undef QCHUNK
    const float cs = 0.12751743082459868f;
    const auto s0 = __builtin_amdgcn_permlane16_swap(cvtpk(acc0[0] * cs, acc0[1] * cs), cvtpk(acc1[0] * cs, acc1[1] * cs), false, false);
    const auto s1 = __builtin_amdgcn_permlane16_swap(cvtpk(acc0[2] * cs, acc0[3] * cs), cvtpk(acc1[2] * cs, acc1[3] * cs), false, false);
    qfrag = u32x4{s0[0], s1[0], s0[1], s1[1]};
  }
  STAMP(1);
  const u32 cflag_v = lane < 4 ? lane * 512 : 0x7fffffffu;
  const u32 cflag_s = WS_FLAG + (((bp * 2 + (w >> 2)) * 8 + quad) * 4) * 512 + (e * 4 + wl) * 4;
  st16<0>(qfrag, lane * 16, rw, WS_Q + ((((bp * 2 + (w >> 2)) * 32 + quad * 4) * 8 + e * 4 + wl) << 10));
  bf16x8 qf[8];
  if (isB) {
#pragma unroll 1
    for (u32 hc = 2; hc < 8; ++hc) {
      const u32 off = (hc >> 1) * SLOT + (hc & 1) * 4096, m = lb + wl * 8192 + off;
      waitvm<7>(); dma<0, KV_NT>(m, vK, rk, kvbase + off);
      waitvm<7>(); dma<1024, KV_NT>(m, vK ^ 64, rk, kvbase + off);
      waitvm<7>(); dma<2048, KV_NT>(m, vK ^ 128, rk, kvbase + off);
      waitvm<7>();
      if (hc == 3) st4(1u, cflag_v, rw, cflag_s);
      dma<3072, KV_NT>(m, vK ^ 192, rk, kvbase + off);
      if (hc == 5) barvm<8>();
    }
#pragma unroll 1
    for (u32 hc = 0; hc < 2; ++hc) {
      const u32 m = lb + 4 * SLOT + wl * 8192 + hc * 4096, so = kvbase + hc * 4096;
      waitvm<7>(); dma<0, KV_NT>(m, vV, rv, so);
      waitvm<7>(); dma<1024, KV_NT>(m, vV, rv, so);
      waitvm<7>(); dma<2048, KV_NT>(m, vV, rv, so);
      waitvm<7>(); dma<3072, KV_NT>(m, vV, rv, so);
    }
    STAMP(2);
    bar();
    barvm<8>();
#pragma unroll
    for (int d = 0; d < 8; ++d) qf[d] = ldsb128(lb + wl * 8192 + d * 1024 + lane * 16);
  } else {
    waitvm<0>();
    st4(1u, cflag_v, rw, cflag_s);
    bar();
    poll((wl * 8 + (lane & 7)) * 4, rw, WS_FLAG + L * 512);
    STAMP(2);
    u32x4 t[8];
    const u32 so = WS_Q + (((b * 32 + quad * 4 + wl) * 8) << 10);
    ld16<0>(t[0], lane * 16, rw, so); ld16<1024>(t[1], lane * 16, rw, so); ld16<2048>(t[2], lane * 16, rw, so); ld16<3072>(t[3], lane * 16, rw, so);
    ld16<0>(t[4], lane * 16, rw, so + 4096); ld16<1024>(t[5], lane * 16, rw, so + 4096); ld16<2048>(t[6], lane * 16, rw, so + 4096); ld16<3072>(t[7], lane * 16, rw, so + 4096);
    asm volatile("s_waitcnt vmcnt(0)" : "+v"(t[0]), "+v"(t[1]), "+v"(t[2]), "+v"(t[3]), "+v"(t[4]), "+v"(t[5]), "+v"(t[6]), "+v"(t[7])::"memory");
#pragma unroll
    for (int d = 0; d < 8; ++d) qf[d] = __builtin_bit_cast(bf16x8, t[d]);
  }
  STAMP(3);
  f32x16 S[8];
  float mx;
  {
    const u32 aK = lb + (isB ? 2 * SLOT : 0) + r32 * 256 + ((h2 ^ l15) << 4);
    float m0 = -__builtin_inff(), m1 = -__builtin_inff();
    constexpr int PD = 6;
    bf16x8 kf[PD];
#pragma unroll
    for (int n = 0; n < PD; ++n) kf[n] = ldsb128((aK ^ ((n & 7) * 32)) + (n >> 3) * 8192);
    PIN();
#pragma unroll
    for (int n = 0; n < 64; ++n) {
      const int t = n >> 3, d = n & 7;
      if (d == 0) S[t] = f32x16{};
      S[t] = __builtin_amdgcn_mfma_f32_32x32x16_bf16(kf[n % PD], qf[d], S[t], 0, 0, 0);
      if (n + PD < 64) kf[n % PD] = ldsb128((aK ^ (((n + PD) & 7) * 32)) + ((n + PD) >> 3) * 8192);
      if (t > 0 && d >= 4) { const int i = (d - 4) * 4; m0 = max3f(m0, S[t - 1][i], S[t - 1][i + 1]); m1 = max3f(m1, S[t - 1][i + 2], S[t - 1][i + 3]); asm volatile("" : "+v"(m0), "+v"(m1)); }
      PIN();
      if (n == 31) {
        asm volatile("s_barrier" ::: "memory");
        if (!isB) {
#pragma unroll
          for (int dd = 0; dd < 8; ++dd) *(LAS bf16x8*)(size_t)(lb + wl * 8192 + dd * 1024 + lane * 16) = qf[dd];
        }
        PIN();
      }
    }
    barvm<0>();
    PIN();
#pragma unroll
    for (int i = 0; i < 16; i += 4) { m0 = max3f(m0, S[7][i], S[7][i + 1]); m1 = max3f(m1, S[7][i + 2], S[7][i + 3]); }
    mx = max2f(m0, m1);
    const auto sw = __builtin_amdgcn_permlane32_swap(__float_as_uint(mx), __float_as_uint(mx), false, false);
    mx = max2f(__uint_as_float(sw[0]), __uint_as_float(sw[1]));
  }
  STAMP(4);
  st4(1u, cflag_v, rw, cflag_s);
  u32x4 P[16];
  float ls, mA = 0.f, lA = 0.f;
  {
    float l0 = 0.f, l1 = 0.f, l2 = 0.f, l3 = 0.f;
#pragma unroll
    for (int hf = 0; hf < 2; ++hf) {
      const u32 ch = isB ? 3 : 1 + hf, m = lb + (isB ? 2 : 1 - hf) * SLOT + wl * 8192, so = kvbase + ch * SLOT;
      const bool en = !(isB && hf == 1);
      if (hf == 1 && isB) { mA = *(const LAS float*)(size_t)(lb + 3 * SLOT + wl * 256 + r32 * 4); lA = *(const LAS float*)(size_t)(lb + 3 * SLOT + wl * 256 + 128 + r32 * 4); }
#pragma unroll
      for (int u = 0; u < 8; ++u) {
        if (en) {
          if ((u & 3) == 0) dma<0, KV_NT>(m + (u >> 2) * 4096, vV, rv, so + (u >> 2) * 4096);
          if ((u & 3) == 1) dma<1024, KV_NT>(m + (u >> 2) * 4096, vV, rv, so + (u >> 2) * 4096);
          if ((u & 3) == 2) dma<2048, KV_NT>(m + (u >> 2) * 4096, vV, rv, so + (u >> 2) * 4096);
          if ((u & 3) == 3) dma<3072, KV_NT>(m + (u >> 2) * 4096, vV, rv, so + (u >> 2) * 4096);
        }
        PIN();
        const int t = hf * 4 + (u >> 1), o = (u & 1) * 8;
        float p[8];
#pragma unroll
        for (int i = 0; i < 8; ++i) p[i] = __builtin_amdgcn_exp2f(S[t][o + i] - mx);
        l0 += p[0]; l1 += p[1]; l2 += p[2]; l3 += p[3]; l0 += p[4]; l1 += p[5]; l2 += p[6]; l3 += p[7];
        P[hf * 8 + u] = u32x4{cvtpk(p[0], p[1]), cvtpk(p[2], p[3]), cvtpk(p[4], p[5]), cvtpk(p[6], p[7])};
        asm volatile("" : "+v"(P[hf * 8 + u]), "+v"(l0), "+v"(l1), "+v"(l2), "+v"(l3));
        PIN();
      }
      bar();
    }
    ls = (l0 + l1) + (l2 + l3);
    const auto sw = __builtin_amdgcn_permlane32_swap(__float_as_uint(ls), __float_as_uint(ls), false, false);
    ls = __uint_as_float(sw[0]) + __uint_as_float(sw[1]);
  }
  STAMP(5);
  if (!isB) *(LAS float*)(size_t)(lb + 3 * SLOT + wl * 256 + lane * 4) = h2 ? ls : mx;
  f32x16 O[4] = {};
  f32x16 Dm[4];
  float fB = 0.f, stat = 0.f;
  {
    const u32 aV = lb + (4 * h2 + q4) * 256 + (q4 << 6) + (g4 & 1) * 32 + p4 * 8;
#pragma unroll
    for (int hf = 0; hf < 2; ++hf) {
      const u32 vb = (isB ? 2 * hf : 4 - 3 * hf) * SLOT;
      constexpr int PD = 6;
      bf16x8 vf[PD];
#pragma unroll
      for (int n = 0; n < PD; ++n) { const u32 a = (aV ^ ((n & 3) << 6)) + vb + (n >> 2) * 4096; vf[n] = ldstr(a, a + 2048); }
      PIN();
#pragma unroll
      for (int n = 0; n < 32; ++n) {
        O[n & 3] = __builtin_amdgcn_mfma_f32_32x32x16_bf16(vf[n % PD], __builtin_bit_cast(bf16x8, P[hf * 8 + (n >> 2)]), O[n & 3], 0, 0, 0);
        if (n + PD < 32) { const u32 a = (aV ^ (((n + PD) & 3) << 6)) + vb + ((n + PD) >> 2) * 4096; vf[n % PD] = ldstr(a, a + 2048); }
        PIN();
      }
      if (hf == 0) {
        barvm<8>();
        STAMP(6);
        if (isB) {
          const float M = max2f(mA, mx), fa = __builtin_amdgcn_exp2f(mA - M), fb = __builtin_amdgcn_exp2f(mx - M);
          const float Ls = fa * lA + fb * ls, inv = 1.0f / Ls, fA = fa * inv;
          fB = fb * inv;
          stat = M + __builtin_amdgcn_logf(Ls);
          const u32 da = lb + (wl < 2 ? 4 : 1) * SLOT + (wl & 1) * 16384 + lane * 16;
#pragma unroll
          for (int dt = 0; dt < 4; ++dt)
#pragma unroll
            for (int i = 0; i < 4; ++i) {
              const f32x4 v = *(const LAS f32x4*)(size_t)(da + (dt * 4 + i) * 1024);
              Dm[dt][4 * i] = v[0] * fA; Dm[dt][4 * i + 1] = v[1] * fA; Dm[dt][4 * i + 2] = v[2] * fA; Dm[dt][4 * i + 3] = v[3] * fA;
            }
          asm volatile("" : "+v"(Dm[0]), "+v"(Dm[1]), "+v"(Dm[2]), "+v"(Dm[3]));
          barvm<0>();
          STAMP(7);
        }
      }
    }
  }
  STAMP(8);
  if (!isB) {
    barvm<0>();
    const u32 da = lb + (wl < 2 ? 4 : 1) * SLOT + (wl & 1) * 16384 + lane * 16;
#pragma unroll
    for (int dt = 0; dt < 4; ++dt)
#pragma unroll
      for (int i = 0; i < 4; ++i) *(LAS f32x4*)(size_t)(da + (dt * 4 + i) * 1024) = f32x4{O[dt][4 * i], O[dt][4 * i + 1], O[dt][4 * i + 2], O[dt][4 * i + 3]};
    bar();
    const u32 vW0 = h2 * 2048 + ((r32 ^ (h2 << 2)) << 4), vW1 = h2 * 2048 + 3072 + ((r32 ^ ((2 + h2) << 2)) << 4);
    u32 so = ((quad * 4 + par * 2) * 128 + 16 * wl) * 2048 + s * 512;
#pragma unroll 1
    for (u32 c = 0; c < 4; ++c) {
      const u32 m = lb + ((0x1403u >> (4 * c)) & 7) * SLOT + wl * 8192;
#pragma unroll
      for (int i = 0; i < 4; ++i) {
        waitvm<7>(); dma<0, 0>(m + i * 2048, vW0, ro, so + i * 8192);
        waitvm<7>(); dma<1024, 0>(m + i * 2048, vW1, ro, so + i * 8192);
      }
      so += 64 * 2048;
      if (c == 0) bar();
    }
    STAMP(9);
    barvm<0>();
  } else {
    const u32 hs = (b * 32 + quad * 4 + wl) * 4 + s;
#pragma unroll
    for (int dt = 0; dt < 4; ++dt)
#pragma unroll
      for (int u = 0; u < 2; ++u) {
        float o[8];
#pragma unroll
        for (int i = 0; i < 8; ++i) o[i] = fmaf(O[dt][8 * u + i], fB, Dm[dt][8 * u + i]);
        const u32x4 ov = {cvtpk(o[0], o[1]), cvtpk(o[2], o[3]), cvtpk(o[4], o[5]), cvtpk(o[6], o[7])};
        if (u == 0) st16<0>(ov, lane * 16, rw, WS_P + hs * 8192 + dt * 2048); else st16<1024>(ov, lane * 16, rw, WS_P + hs * 8192 + dt * 2048);
      }
    st4(__float_as_uint(stat), lane < 32 ? lane * 4 : 0x7fffffffu, rw, WS_STAT + hs * 128);
    waitvm<0>();
    st4(1u, cflag_v, rw, WS_FLAG + (((bp * 2 + (wl >> 1)) * 8 + quad) * 4) * 512 + 128 + ((par * 2 + (wl & 1)) * 4 + s) * 4);
    STAMP(9);
    bar();
  }
  STAMP(10);
  f32x16 acc[2] = {};
  {
    poll(((w >> 1) * 4 + p4) * 4, rw, WS_FLAG + L * 512 + 128);
    STAMP(11);
    const u32 hs = ((bp * 2 + (w >> 2)) * 32 + quad * 4 + par * 2 + ((w >> 1) & 1)) * 4;
    u32 st[4];
    u32x4 G[4][4];
    ld4<0>(st[0], r32 * 4, rw, WS_STAT + hs * 128); ld4<128>(st[1], r32 * 4, rw, WS_STAT + hs * 128);
    ld4<256>(st[2], r32 * 4, rw, WS_STAT + hs * 128); ld4<384>(st[3], r32 * 4, rw, WS_STAT + hs * 128);
    const u32 so = WS_P + hs * 8192 + (w & 1) * 4096;
#pragma unroll
    for (int sp = 0; sp < 4; ++sp) ld16<0>(G[0][sp], lane * 16, rw, so + sp * 8192);
#pragma unroll
    for (int sp = 0; sp < 4; ++sp) ld16<1024>(G[1][sp], lane * 16, rw, so + sp * 8192);
#pragma unroll
    for (int sp = 0; sp < 4; ++sp) ld16<2048>(G[2][sp], lane * 16, rw, so + sp * 8192);
#pragma unroll
    for (int sp = 0; sp < 4; ++sp) ld16<3072>(G[3][sp], lane * 16, rw, so + sp * 8192);
    bf16x8 Wf[4][4];
    const u32 p4s = ((p4 & 1) << 1) | (p4 >> 1);
    const u32 aW = lb + (4 * h2 + q4) * 512 + ((w ^ q4) << 6) + (g4 & 1) * 32 + p4s * 8;
#pragma unroll
    for (int c = 0; c < 4; ++c)
#pragma unroll
      for (int i = 0; i < 4; ++i) {
        const u32 a = aW + ((0x1403u >> (4 * c)) & 7) * SLOT + i * 8192;
        Wf[c][i] = ldstr(a, a + 4096);
      }
    asm volatile("s_waitcnt vmcnt(16)" : "+v"(st[0]), "+v"(st[1]), "+v"(st[2]), "+v"(st[3])::"memory");
    float wt[4];
    {
      const float s0 = __uint_as_float(st[0]), s1 = __uint_as_float(st[1]), s2 = __uint_as_float(st[2]), s3 = __uint_as_float(st[3]);
      const float M = max2f(max2f(s0, s1), max2f(s2, s3));
      wt[0] = __builtin_amdgcn_exp2f(s0 - M); wt[1] = __builtin_amdgcn_exp2f(s1 - M); wt[2] = __builtin_amdgcn_exp2f(s2 - M); wt[3] = __builtin_amdgcn_exp2f(s3 - M);
      const float inv = 1.0f / ((wt[0] + wt[1]) + (wt[2] + wt[3]));
      wt[0] *= inv; wt[1] *= inv; wt[2] *= inv; wt[3] *= inv;
    }
#pragma unroll
    for (int i = 0; i < 4; ++i) {
      if (i == 0) asm volatile("s_waitcnt vmcnt(12)" : "+v"(G[0][0]), "+v"(G[0][1]), "+v"(G[0][2]), "+v"(G[0][3])::"memory");
      if (i == 1) asm volatile("s_waitcnt vmcnt(8)" : "+v"(G[1][0]), "+v"(G[1][1]), "+v"(G[1][2]), "+v"(G[1][3])::"memory");
      if (i == 2) asm volatile("s_waitcnt vmcnt(4)" : "+v"(G[2][0]), "+v"(G[2][1]), "+v"(G[2][2]), "+v"(G[2][3])::"memory");
      if (i == 3) asm volatile("s_waitcnt vmcnt(0)" : "+v"(G[3][0]), "+v"(G[3][1]), "+v"(G[3][2]), "+v"(G[3][3])::"memory");
      u32x4 o;
#pragma unroll
      for (int d = 0; d < 4; ++d) {
        float lo = bflo(G[i][0][d]) * wt[0], hi = bfhi(G[i][0][d]) * wt[0];
#pragma unroll
        for (int sp = 1; sp < 4; ++sp) { lo = fmaf(bflo(G[i][sp][d]), wt[sp], lo); hi = fmaf(bfhi(G[i][sp][d]), wt[sp], hi); }
        o[d] = cvtpk(lo, hi);
      }
      *(LAS u32x4*)(size_t)(lb + 2 * SLOT + (i * 8 + w) * 1024 + lane * 16) = o;
      bar();
#pragma unroll
      for (int c = 0; c < 4; ++c)
#pragma unroll
        for (int rb = 0; rb < 2; ++rb)
          acc[rb] = __builtin_amdgcn_mfma_f32_32x32x16_bf16(Wf[c][i], ldsb128(lb + 2 * SLOT + (i * 8 + rb * 4 + c) * 1024 + lane * 16), acc[rb], 0, 0, 0);
    }
  }
  STAMP(12);
  {
    const u32 src = quad * 2 + par, Ld = ((bp * 2) * 8 + w) * 4 + s;
#pragma unroll
    for (int rb = 0; rb < 2; ++rb)
#pragma unroll
      for (int t = 0; t < 2; ++t) {
        const u32x4 ov = {cvtpk(acc[rb][8 * t], acc[rb][8 * t + 1]), cvtpk(acc[rb][8 * t + 2], acc[rb][8 * t + 3]),
                          cvtpk(acc[rb][8 * t + 4], acc[rb][8 * t + 5]), cvtpk(acc[rb][8 * t + 6], acc[rb][8 * t + 7])};
        if (t == 0) st16<0>(ov, (r32 * 2 + h2) * 16, rw, WS_T + ((Ld + 32 * rb) * 16 + src) * 2048);
        else st16<1024>(ov, (r32 * 2 + h2) * 16, rw, WS_T + ((Ld + 32 * rb) * 16 + src) * 2048);
      }
    waitvm<0>();
    const u32 gflag_v = lane < 2 ? lane * (32 * 512) : 0x7fffffffu, gflag_s = WS_FLAG + Ld * 512 + 256 + src * 4;
    st4(1u, gflag_v, rw, gflag_s);
    STAMP(13);
    poll(l15 * 4, rw, WS_FLAG + L * 512 + 256);
    STAMP(14);
    u32x4 T[4];
    const u32 vo = g4 * 2048 + w * 256 + l15 * 16, so = WS_T + L * 32768;
#pragma unroll
    for (int i = 0; i < 4; ++i) ld16<0>(T[i], vo, rw, so + i * 8192);
    st4(1u, gflag_v, rw, gflag_s);
    asm volatile("s_waitcnt vmcnt(1)" : "+v"(T[0]), "+v"(T[1]), "+v"(T[2]), "+v"(T[3])::"memory");
    float v[8];
#pragma unroll
    for (int d = 0; d < 4; ++d) {
      v[2 * d] = (bflo(T[0][d]) + bflo(T[1][d])) + (bflo(T[2][d]) + bflo(T[3][d]));
      v[2 * d + 1] = (bfhi(T[0][d]) + bfhi(T[1][d])) + (bfhi(T[2][d]) + bfhi(T[3][d]));
    }
    float u[4], z[2];
#pragma unroll
    for (int i = 0; i < 4; ++i) {
      const auto sw = __builtin_amdgcn_permlane32_swap(__float_as_uint(v[i]), __float_as_uint(v[i + 4]), false, false);
      u[i] = __uint_as_float(sw[0]) + __uint_as_float(sw[1]);
    }
#pragma unroll
    for (int i = 0; i < 2; ++i) {
      const auto sw = __builtin_amdgcn_permlane16_swap(__float_as_uint(u[i]), __float_as_uint(u[i + 2]), false, false);
      z[i] = __uint_as_float(sw[0]) + __uint_as_float(sw[1]);
    }
    __builtin_amdgcn_raw_buffer_store_b32(cvtpk(z[0], z[1]), rout, (l15 >> 1) * 2048 + (l15 & 1) * 16 + g4 * 4,
                                          (b * 32 + wl * 8) * 2048 + (s * 256 + quad * 32 + (w >> 2) * 16) * 2, 16);
  }
}

extern "C" void kernel_launch(void* const* d_in, const int* in_sizes, int n_in, void* d_out, int out_size, void* d_ws, size_t ws_size,
                              hipStream_t stream) {
  (void)hipFuncSetAttribute((const void*)fused_kernel, hipFuncAttributeMaxDynamicSharedMemorySize, 163840);
  fused_kernel<<<256, 512, 163840, stream>>>(d_in[0], d_in[1], d_in[2], d_in[3], d_in[4], d_out, d_ws);
}
```

```cpp
#include <hip/hip_runtime.h>
#include <utility>

typedef unsigned u32;
typedef unsigned long long u64;
typedef u32 u32x2 __attribute__((ext_vector_type(2)));
typedef u32 u32x4 __attribute__((ext_vector_type(4)));
typedef float f32x2 __attribute__((ext_vector_type(2)));
typedef float f32x4 __attribute__((ext_vector_type(4)));
typedef float f32x16 __attribute__((ext_vector_type(16)));
typedef __bf16 bf16x2 __attribute__((ext_vector_type(2)));
typedef __bf16 bf16x4 __attribute__((ext_vector_type(4)));
typedef __bf16 bf16x8 __attribute__((ext_vector_type(8)));
typedef __amdgpu_buffer_rsrc_t rsrc_t;
#define LAS __attribute__((address_space(3)))
#define GAS __attribute__((address_space(1)))
#define DI __device__ __forceinline__
#define PIN() __builtin_amdgcn_sched_barrier(0)

#define KV_NT 1
#define SPOLL 1
#define TSWZ 0
#define PRIME 1
#define QDEPTH 10
#define PRIO 0
#define STAMPS 0

constexpr u32 WS_FLAG = 0;
constexpr u32 WS_STAT = 1u << 18;
constexpr u32 WS_Q = 1u << 20;
constexpr u32 WS_P = 4u << 20;
constexpr u32 WS_T = 12u << 20;
constexpr u32 WS_DBG = 20u << 20;
constexpr u32 WS_END = 21u << 20;
constexpr u32 SLOT = 32768;
constexpr u32 OOB = 0x7ffffff0u;
constexpr u32 SPIN_CAP = 1u << 18;
constexpr u32 CODE_LINES = 180;

DI u32 cvtpk(float lo, float hi) { f32x2 v = {lo, hi}; return __builtin_bit_cast(u32, __builtin_convertvector(v, bf16x2)); }
DI float max2f(float a, float b) { return __builtin_elementwise_maximum(a, b); }
DI float max3f(float a, float b, float c) { return max2f(max2f(a, b), c); }
DI float bflo(u32 x) { return __uint_as_float(x << 16); }
DI float bfhi(u32 x) { return __uint_as_float(x & 0xffff0000u); }
template <int N> DI void waitvm() { asm volatile("s_waitcnt vmcnt(%0)" ::"i"(N) : "memory"); }
DI void bar() { asm volatile("s_waitcnt lgkmcnt(0)\n\ts_barrier" ::: "memory"); }
template <int N> DI void barvm() { asm volatile("s_waitcnt vmcnt(%0) lgkmcnt(0)\n\ts_barrier" ::"i"(N) : "memory"); }
template <int IMM, int NT> DI void dma(u32 m0v, u32 voff, rsrc_t rs, u32 soff) {
  if constexpr (NT) asm volatile("s_mov_b32 m0, %0\n\ts_nop 0\n\tbuffer_load_dwordx4 %1, %2, %3 offen offset:%4 nt lds" ::"s"(m0v), "v"(voff), "s"(rs), "s"(soff), "i"(IMM) : "memory");
  else asm volatile("s_mov_b32 m0, %0\n\ts_nop 0\n\tbuffer_load_dwordx4 %1, %2, %3 offen offset:%4 lds" ::"s"(m0v), "v"(voff), "s"(rs), "s"(soff), "i"(IMM) : "memory");
}
template <int IMM> DI void dma_sc1(u32 m0v, u32 voff, rsrc_t rs, u32 soff) {
  asm volatile("s_mov_b32 m0, %0\n\ts_nop 0\n\tbuffer_load_dwordx4 %1, %2, %3 offen offset:%4 sc1 lds" ::"s"(m0v), "v"(voff), "s"(rs), "s"(soff), "i"(IMM) : "memory");
}
template <class F, int... I> DI void sfor_(F&& f, std::integer_sequence<int, I...>) { (f(std::integral_constant<int, I>{}), ...); }
template <int N, class F> DI void sfor(F&& f) { sfor_(f, std::make_integer_sequence<int, N>{}); }
#define SFOR(N_, n_) sfor<N_>([&](auto n_##c) __attribute__((always_inline)) { constexpr int n_ = n_##c;
#define SEND });
DI void rawbar() { asm volatile("s_barrier" ::: "memory"); }
template <int IMM> DI void ld16(u32x4& d, u32 voff, rsrc_t rs, u32 soff) {
  asm volatile("buffer_load_dwordx4 %0, %1, %2, %3 offen offset:%4 sc1" : "=v"(d) : "v"(voff), "s"(rs), "s"(soff), "i"(IMM) : "memory");
}
template <int IMM> DI void ld4(u32& d, u32 voff, rsrc_t rs, u32 soff) {
  asm volatile("buffer_load_dword %0, %1, %2, %3 offen offset:%4 sc1" : "=v"(d) : "v"(voff), "s"(rs), "s"(soff), "i"(IMM) : "memory");
}
template <int IMM> DI void st16(u32x4 v, u32 voff, rsrc_t rs, u32 soff) {
  asm volatile("buffer_store_dwordx4 %0, %1, %2, %3 offen offset:%4 sc1\n\ts_nop 1" ::"v"(v), "v"(voff), "s"(rs), "s"(soff), "i"(IMM) : "memory");
}
DI void st4(u32 v, u32 voff, rsrc_t rs, u32 soff) { __builtin_amdgcn_raw_buffer_store_b32(v, rs, voff, soff, 16); }
DI void poll(u32 voff, rsrc_t rs, u32 soff) {
  for (u32 n = 0; n < SPIN_CAP; ++n) {
    u32 v;
    asm volatile("buffer_load_dword %0, %1, %2, %3 offen sc1\n\ts_waitcnt vmcnt(0)" : "=&v"(v) : "v"(voff), "s"(rs), "s"(soff) : "memory");
    if (__all(v == 1u)) break;
  }
}
typedef u32 u32x8 __attribute__((ext_vector_type(8)));
DI void poll8(const char* wsp, rsrc_t rs, u32 off, u32 lane) {
  const char* p = wsp + off;
  for (u32 n = 0; n < SPIN_CAP; ++n) {
    u32x8 v;
    asm volatile("s_load_dwordx8 %0, %1, 0x0 glc\n\ts_waitcnt lgkmcnt(0)" : "=&s"(v) : "s"(p) : "memory");
    if ((v[0] & v[1] & v[2] & v[3] & v[4] & v[5] & v[6] & v[7]) == 1u) break;
  }
}
DI void poll4(const char* wsp, rsrc_t rs, u32 off, u32 lane) {
  const char* p = wsp + off;
  for (u32 n = 0; n < SPIN_CAP; ++n) {
    u32x4 v;
    asm volatile("s_load_dwordx4 %0, %1, 0x0 glc\n\ts_waitcnt lgkmcnt(0)" : "=&s"(v) : "s"(p) : "memory");
    if ((v[0] & v[1] & v[2] & v[3]) == 1u) break;
  }
}
DI float pinf(float x) { asm volatile("" : "+v"(x)); return x; }
template <int IMM> DI void ld8(u32x2& d, u32 voff, rsrc_t rs, u32 soff) {
  asm volatile("buffer_load_dwordx2 %0, %1, %2, %3 offen offset:%4 sc1" : "=v"(d) : "v"(voff), "s"(rs), "s"(soff), "i"(IMM) : "memory");
}
DI void prio(int p) {
}
DI bf16x8 ldsb128(u32 a) { return *(const LAS bf16x8*)(size_t)a; }
DI bf16x8 ldstr(u32 a0, u32 a1) {
  bf16x4 lo = __builtin_amdgcn_ds_read_tr16_b64_v4bf16((LAS bf16x4*)(size_t)a0);
  bf16x4 hi = __builtin_amdgcn_ds_read_tr16_b64_v4bf16((LAS bf16x4*)(size_t)a1);
  return __builtin_shufflevector(lo, hi, 0, 1, 2, 3, 4, 5, 6, 7);
}
DI rsrc_t mkrs(const void* p, u32 bytes) { return __builtin_amdgcn_make_buffer_rsrc((void*)p, 0, bytes, 0x00020000); }

#define STAMP(i)

__global__ __launch_bounds__(512, 2) void fused_kernel(const void* px, const void* pk, const void* pv, const void* pwq, const void* pwo, void* pout, void* pws) {
  extern __shared__ __attribute__((aligned(16))) char lds_raw[];
  STAMP(0);
  const u32 tid = threadIdx.x, lane = tid & 63;
  u32 w = __builtin_amdgcn_readfirstlane(tid >> 6);
  asm volatile("s_nop 4" : "+s"(w));
  const u32 bid = blockIdx.x, xcd = bid & 7, jj = bid >> 3;
  const u32 wl = w & 3;
  const bool isB = w >= 4;
  const u32 lb = (u32)(size_t)(LAS char*)lds_raw;
  {
    u64 pc;
    asm volatile("s_getpc_b64 %0" : "=s"(pc));
    const u32 ln = (jj & 15) * 16 + w * 2 + (lane & 1);
    const char* cp = (const char*)(pc & ~127ull) + (ln < CODE_LINES ? ln : CODE_LINES - 1) * 128u;
    asm volatile("s_mov_b32 m0, %0\n\ts_nop 0\n\tglobal_load_lds_dword %1, off" ::"s"(lb + w * 256), "v"(cp) : "memory");
  }
  const u32 kvh = xcd & 3, bq = xcd >> 2, hq = (jj >> 2) & 1, s = jj & 3, par = (jj >> 3) & 1, e = par * 4 + s;
  const u32 bpa = bq * 2 + (jj >> 4), ba = bpa * 2 + par, qa = kvh * 2 + hq;
  const u32 bp = (PRIME ? hq : bq) * 2 + (jj >> 4), b = bp * 2 + par, quad = kvh * 2 + (PRIME ? bq : hq), L = (b * 8 + quad) * 4 + s;
  const u32 l15 = lane & 15, g4 = lane >> 4;
  const u32 swz8 = ((lane & 7) ^ ((g4 & 1) << 2)) << 4;
  asm volatile("" : "+s"(px), "+s"(pk), "+s"(pv), "+s"(pwq), "+s"(pwo), "+s"(pout), "+s"(pws));
  STAMP(7);
  const rsrc_t rx = mkrs(px, 256u * 1024 * 2), rk = mkrs(pk, 8u * 4 * 8192 * 256), rv = mkrs(pv, 8u * 4 * 8192 * 256);
  const rsrc_t rq = mkrs(pwq, 1024u * 4096 * 2), ro = mkrs(pwo, 4096u * 1024 * 2), rw = mkrs(pws, WS_END), rout = mkrs(pout, 256u * 1024 * 2);
  const u32 r32 = lane & 31, h2 = lane >> 5, q4 = l15 >> 2, p4 = lane & 3, p4s = ((p4 & 1) << 1) | (p4 >> 1);
  const u32 kv0 = ((ba * 4 + kvh) * 8192 + 6144 + s * 512) * 256;
  const u32 kvbase = kv0 + wl * 8192;
  const u32 vV = g4 * 256 + ((l15 ^ (g4 << 2)) << 4);
  const u32 hello_v = lane < 16 ? ((lane & 1) * 32 + (lane >> 1) * 4) * 1024 : OOB, hello_s = WS_FLAG + (bp * 64 + s) * 1024 + 512 + (quad * 2 + par) * 4;
  const u32 cflag_v = lane < 4 ? lane * 1024 : OOB;
  const u32 cflag_s = WS_FLAG + (bpa * 64 + (wl >> 1) * 32 + qa * 4) * 1024 + (e >> 2) * 128 + ((e & 3) * 2 + (wl & 1)) * 4;
  const u32 qso = WS_Q + (((ba * 32 + qa * 4 + wl) * 8) << 10);
  const u32 aK = lb + (isB ? 2 * SLOT : 0) + r32 * 256 + ((h2 ^ l15) << 4);
  u32 dump = lb + 4 * SLOT + wl * 8192 + lane * 16;
  constexpr int PD = 6;
  u32x4 qf[8];
  bf16x8 kf[PD];
  u32 hello = 1u;
#define QLOAD() do { ld16<0>(qf[0], lane * 16, rw, qso); ld16<1024>(qf[1], lane * 16, rw, qso); ld16<2048>(qf[2], lane * 16, rw, qso); ld16<3072>(qf[3], lane * 16, rw, qso); \
    ld16<0>(qf[4], lane * 16, rw, qso + 4096); ld16<1024>(qf[5], lane * 16, rw, qso + 4096); ld16<2048>(qf[6], lane * 16, rw, qso + 4096); ld16<3072>(qf[7], lane * 16, rw, qso + 4096); } while (0)
#define QOPS "+v"(qf[0]), "+v"(qf[1]), "+v"(qf[2]), "+v"(qf[3]), "+v"(qf[4]), "+v"(qf[5]), "+v"(qf[6]), "+v"(qf[7])
  if (__builtin_expect(isB, 1)) {
    const u32 vQ = (lane >> 3) * 8192 + swz8, vX = g4 * 2048 + ((l15 ^ g4) << 4);
    u32 sW = (quad * 512 + e * 64) * 2 + wl * 32 * 8192, sX = (bp * 64 + wl * 16) * 2048;
    u32 m = lb + 2 * SLOT + wl * 4096;
#pragma unroll 1
    for (u32 c = 0; c < 8; ++c) {
#pragma unroll
      for (int i = 0; i < 4; ++i) { waitvm<QDEPTH - 1>(); if (QDEPTH - 9 == i && c > 1) rawbar(); dma<0, 0>(m + 16384 + i * 1024, vQ, rq, sW + i * 8 * 8192); }
#pragma unroll
      for (int i = 0; i < 4; ++i) { waitvm<QDEPTH - 1>(); dma<0, 0>(m + i * 1024, vX ^ (64 * i), rx, sX + i * 4 * 2048); }
      sW += 128 * 8192; sX += 256;
      m = m >= lb + 4 * SLOT ? m - 4 * SLOT : m + SLOT;
    }
    barvm<8>();
    const u32 vK = g4 * 256 + ((l15 ^ g4) << 4);
#pragma unroll 1
    for (u32 hc = 0; hc < 8; ++hc) {
      const u32 off = (hc >> 1) * SLOT + (hc & 1) * 4096, mk = lb + wl * 8192 + off, so = kvbase + off;
      waitvm<7>(); dma<0, KV_NT>(mk, vK, rk, so);
      waitvm<7>(); dma<1024, KV_NT>(mk, vK ^ 64, rk, so);
      waitvm<7>(); dma<2048, KV_NT>(mk, vK ^ 128, rk, so);
      waitvm<7>();
      if (hc - 1 < 2) rawbar();
      dma<3072, KV_NT>(mk, vK ^ 192, rk, so);
    }
    STAMP(3);
    rawbar();
    rawbar();
#pragma unroll
    for (int d = 0; d < 8; ++d) qf[d] = *(const LAS u32x4*)(size_t)(dump + d * 1024);
    asm volatile("" : QOPS::"memory");
#pragma unroll 1
    for (u32 hc = 0; hc < 4; ++hc) {
      if (!(hc & 1)) rawbar();
      const u32 mv = lb + (hc < 2 ? 4 * SLOT : 0) + wl * 8192 + (hc & 1) * 4096, so = kvbase + (hc >> 1) * SLOT + (hc & 1) * 4096;
      waitvm<7>(); dma<0, KV_NT>(mv, vV, rv, so);
      waitvm<7>(); dma<1024, KV_NT>(mv, vV, rv, so);
      waitvm<7>(); dma<2048, KV_NT>(mv, vV, rv, so);
      waitvm<7>(); dma<3072, KV_NT>(mv, vV, rv, so);
    }
    STAMP(5);
#pragma unroll
    for (int n = 0; n < PD; ++n) kf[n] = ldsb128(aK ^ (n * 32));
    rawbar();
  } else {
    if (w == 0) {
      st16<0>(u32x4{0u, 0u, 0u, 0u}, lane * 16, rw, WS_FLAG + L * 1024);
    } else if (w == 1) {
      asm volatile("s_mov_b32 m0, %0\n\ts_nop 0\n\tbuffer_load_dword %1, %2, %3 offen lds" ::"s"(lb + 2048), "v"(r32 * 8192), "s"(rq),
                   "s"(jj * 32 * 8192 + (quad * 512 + 192) * 2) : "memory");
    }
    const u32 aA = lb + 16384 + (8 * h2 + q4) * 128 + (((wl ^ (q4 >> 1)) & 1) << 6) + (g4 & 1) * 32 + p4s * 8;
    const u32 aB = lb + ((wl >> 1) * 32 + r32) * 256 + ((h2 ^ l15) << 4);
    f32x16 qa_ = {};
    u32 rs_ = 2 * SLOT;
    waitvm<0>();
#pragma unroll 1
    for (u32 c = 0; c < 8; ++c) {
      u32 a = aA + rs_, bb[8];
#pragma unroll
      for (int ks = 0; ks < 8; ++ks) bb[ks] = (aB ^ (ks * 32)) + rs_;
      asm volatile("" : "+v"(a), "+v"(bb[0]), "+v"(bb[1]), "+v"(bb[2]), "+v"(bb[3]), "+v"(bb[4]), "+v"(bb[5]), "+v"(bb[6]), "+v"(bb[7]));
      rawbar();
      if (c == 0) STAMP(4);
      bf16x8 fa[8], fb[8];
#pragma unroll
      for (int ks = 0; ks < 11; ++ks) {
        if (ks >= 3) qa_ = __builtin_amdgcn_mfma_f32_32x32x16_bf16(fa[ks - 3], fb[ks - 3], qa_, 0, 0, 0);
        PIN();
        if (ks < 8) { fa[ks] = ldstr(a + ks * 2048, a + ks * 2048 + 512); fb[ks] = ldsb128(bb[ks]); }
        PIN();
      }
      if (c == 4 && w == 1) st4(1u, hello_v, rw, hello_s);
      rs_ = rs_ == 4 * SLOT ? 0 : rs_ + SLOT;
    }
    const float cs = 0.12751743082459868f;
    const u32 so = WS_Q + ((((bp * 2 + (wl >> 1)) * 32 + quad * 4) * 8 + e * 4 + (wl & 1) * 2) << 10);
    st16<0>(u32x4{cvtpk(qa_[0] * cs, qa_[1] * cs), cvtpk(qa_[2] * cs, qa_[3] * cs), cvtpk(qa_[4] * cs, qa_[5] * cs), cvtpk(qa_[6] * cs, qa_[7] * cs)}, lane * 16, rw, so);
    st16<1024>(u32x4{cvtpk(qa_[8] * cs, qa_[9] * cs), cvtpk(qa_[10] * cs, qa_[11] * cs), cvtpk(qa_[12] * cs, qa_[13] * cs), cvtpk(qa_[14] * cs, qa_[15] * cs)}, lane * 16, rw, so);
    STAMP(1);
    rawbar();
    waitvm<0>();
    st4(1u, cflag_v, rw, cflag_s);
    STAMP(2);
    poll4((const char*)pws, rw, WS_FLAG + L * 1024 + (wl >> 1) * 128 + (wl & 1) * 16, lane);
    STAMP(3);
    const u32 mq = lb + 4 * SLOT + wl * 8192;
    dma_sc1<0>(mq, lane * 16, rw, qso); dma_sc1<1024>(mq, lane * 16, rw, qso); dma_sc1<2048>(mq, lane * 16, rw, qso); dma_sc1<3072>(mq, lane * 16, rw, qso);
    dma_sc1<0>(mq + 4096, lane * 16, rw, qso + 4096); dma_sc1<1024>(mq + 4096, lane * 16, rw, qso + 4096);
    dma_sc1<2048>(mq + 4096, lane * 16, rw, qso + 4096); dma_sc1<3072>(mq + 4096, lane * 16, rw, qso + 4096);
    ld4<512>(hello, w == 1 ? l15 * 4 : OOB, rw, WS_FLAG + L * 1024);
    rawbar();
#pragma unroll
    for (int n = 0; n < PD; ++n) kf[n] = ldsb128(aK ^ (n * 32));
    asm volatile("s_waitcnt vmcnt(0)" : "+v"(hello)::"memory");
#pragma unroll
    for (int d = 0; d < 8; ++d) qf[d] = *(const LAS u32x4*)(size_t)(dump + d * 1024);
    rawbar();
  }
  f32x16 S[8];
  u32x4 P[16];
  float l0 = 0.f, l1 = 0.f, l2 = 0.f, l3 = 0.f;
  STAMP(6);
  prio(1);
  PIN();
#pragma unroll
  for (int n = 0; n < 64; ++n) {
    const int t = n >> 3, d = n & 7;
    if (d == 0) S[t] = f32x16{};
    S[t] = __builtin_amdgcn_mfma_f32_32x32x16_bf16(kf[n % PD], __builtin_bit_cast(bf16x8, qf[d]), S[t], 0, 0, 0);
    if (n + PD < 64) kf[n % PD] = ldsb128((aK ^ (((n + PD) & 7) * 32)) + ((n + PD) >> 3) * 8192);
    if (n >= 12 && n < 60) { const int v = n - 12; S[v >> 4][v & 15] = pinf(__builtin_amdgcn_exp2f(S[v >> 4][v & 15])); }
    if (n >= 13 && n < 61) {
      const int v = n - 13;
      if (v & 1) {
        l1 = pinf(l1 + S[v >> 4][v & 15]);
        u32 pk = cvtpk(S[v >> 4][(v & 15) - 1], S[v >> 4][v & 15]);
        asm volatile("" : "+v"(pk));
        P[v >> 3][(v & 7) >> 1] = pk;
      } else l0 = pinf(l0 + S[v >> 4][v & 15]);
    }
    PIN();
    if (n == 7) { if (!isB) rawbar(); PIN(); }
    if (n == 31) { rawbar(); PIN(); }
    if (n == 59) { barvm<0>(); PIN(); }
  }
  prio(0);
  STAMP(8);
  if (!isB) st4(1u, cflag_v, rw, cflag_s);
  if (w == 1) {
    if (!__all(hello == 1u)) poll(l15 * 4, rw, WS_FLAG + L * 1024 + 512);
    st4(1u, hello_v, rw, hello_s);
  }
  const u32 vW = (lane >> 3) * 2048 + swz8;
  const u32 wso = (quad * 4 + par * 2) * 128 * 2048 + s * 512 + wl * 128;
  float lA = 0.f;
#pragma unroll
  for (int hf = 0; hf < 2; ++hf) {
    const u32 m = lb + (isB ? 3 : 1 + hf) * SLOT + wl * 8192, so = isB ? wso + bp * 32 * 2048 : kvbase + (2 + hf) * SLOT;
    if (hf == 1) {
      rawbar();
      if (isB) {
        lA = *(const LAS float*)(size_t)(lb + 3 * SLOT + wl * 8192 + lane * 4);
        asm volatile("" : "+v"(lA)::"memory");
        dma<0, 0>(m, vW, ro, so);
      }
    }
#pragma unroll
    for (int u = 0; u < 5; ++u) {
      if (isB) {
        if (hf == 0 && u < 4) {
          if (u > 0) dma<0, 0>(m + u * 2048, vW, ro, so + (((2 * u) & 3) * 8 + (u >> 1) * 128) * 2048);
          dma<0, 0>(m + u * 2048 + 1024, vW, ro, so + (((2 * u + 1) & 3) * 8 + (u >> 1) * 128) * 2048);
        }
      } else {
        if (u == 0) { dma<0, KV_NT>(m, vV, rv, so); dma<1024, KV_NT>(m, vV, rv, so); }
        if (u == 1) { dma<2048, KV_NT>(m, vV, rv, so); dma<3072, KV_NT>(m, vV, rv, so); }
        if (u == 2) { dma<0, KV_NT>(m + 4096, vV, rv, so + 4096); dma<1024, KV_NT>(m + 4096, vV, rv, so + 4096); }
        if (u == 3) dma<2048, KV_NT>(m + 4096, vV, rv, so + 4096);
        if (u == 4) dma<3072, KV_NT>(m + 4096, vV, rv, so + 4096);
      }
      PIN();
      const int g = 6 + hf * 5 + u, t = g >> 1, o = (g & 1) * 8;
      float p[8];
#pragma unroll
      for (int i = 0; i < 8; ++i) p[i] = __builtin_amdgcn_exp2f(S[t][o + i]);
      l0 += p[0]; l1 += p[1]; l2 += p[2]; l3 += p[3]; l0 += p[4]; l1 += p[5]; l2 += p[6]; l3 += p[7];
      P[g] = u32x4{cvtpk(p[0], p[1]), cvtpk(p[2], p[3]), cvtpk(p[4], p[5]), cvtpk(p[6], p[7])};
      asm volatile("" : "+v"(P[g]), "+v"(l0), "+v"(l1), "+v"(l2), "+v"(l3));
      PIN();
    }
  }
  float ls = ((l0 + l1) + (l2 + l3)) + lA;
  f32x16 O[4];
  u32 aV[4];
#pragma unroll
  for (int dt = 0; dt < 4; ++dt) { aV[dt] = lb + (isB ? SLOT : 4 * SLOT) + (4 * h2 + q4) * 256 + ((q4 ^ dt) << 6) + (g4 & 1) * 32 + p4 * 8; asm volatile("" : "+v"(aV[dt])); }
  asm volatile("" : "+v"(dump), "+v"(ls));
  bf16x8 vf[PD];
  f32x16 acc[4];
  if (!isB) {
    u32 aV2[4];
#pragma unroll
    for (int dt = 0; dt < 4; ++dt) { aV2[dt] = aV[dt] - 4 * SLOT; asm volatile("" : "+v"(aV2[dt])); }
    STAMP(11);
    rawbar();
    STAMP(12);
    prio(1);
#define VDT(n_) ((n_) < 32 ? (n_) & 3 : ((n_) - 32) >> 3)
#define VKS(n_) ((n_) < 32 ? (n_) >> 2 : 8 + ((n_) & 7))
#define VRD(n_) ldstr(((n_) < 32 ? aV : aV2)[VDT(n_)] + (VKS(n_) & 7) * 4096, ((n_) < 32 ? aV : aV2)[VDT(n_)] + (VKS(n_) & 7) * 4096 + 2048)
#pragma unroll
    for (int n = 0; n < PD; ++n) vf[n] = VRD(n);
    *(LAS float*)(size_t)(lb + 3 * SLOT + wl * 8192 + lane * 4) = ls;
    PIN();
    u32x4 ov;
#pragma unroll
    for (int n = 0; n < 64; ++n) {
      if (n < 4) O[n] = f32x16{};
      O[VDT(n)] = __builtin_amdgcn_mfma_f32_32x32x16_bf16(vf[n % PD], __builtin_bit_cast(bf16x8, P[VKS(n)]), O[VDT(n)], 0, 0, 0);
      if (n + PD < 64) vf[n % PD] = VRD(n + PD);
      if (n >= 43) {
        const int tt = (n - 43) >> 3, k = (n - 43) & 7;
        u32 pk = cvtpk(O[tt][2 * k], O[tt][2 * k + 1]);
        asm volatile("" : "+v"(pk));
        ov[k & 3] = pk;
        if ((k & 3) == 3) *(LAS u32x4*)(size_t)(dump + tt * 2048 + (k >> 2) * 1024) = ov;
      }
      PIN();
      if (n == 31) { rawbar(); PIN(); }
      if (n == 59) { barvm<0>(); PIN(); }
    }
#undef VDT
#undef VKS
#undef VRD
#pragma unroll
    for (int k = 5; k < 8; ++k) ov[k & 3] = cvtpk(O[2][2 * k], O[2][2 * k + 1]);
    *(LAS u32x4*)(size_t)(dump + 5 * 1024) = ov;
#pragma unroll
    for (int f = 0; f < 2; ++f)
      *(LAS u32x4*)(size_t)(dump + (6 + f) * 1024) = u32x4{cvtpk(O[3][8 * f], O[3][8 * f + 1]), cvtpk(O[3][8 * f + 2], O[3][8 * f + 3]), cvtpk(O[3][8 * f + 4], O[3][8 * f + 5]), cvtpk(O[3][8 * f + 6], O[3][8 * f + 7])};
    prio(0);
    STAMP(15);
    bar();
#pragma unroll 1
    for (u32 c = 1; c < 4; ++c) {
      const u32 m = lb + ((0x1403u >> (4 * c)) & 7) * SLOT + wl * 8192, so = wso + ((bp + c) & 3) * 32 * 2048;
#pragma unroll
      for (int p = 0; p < 8; ++p) dma<0, 0>(m + p * 1024, vW, ro, so + ((p & 3) * 8 + (p >> 2) * 128) * 2048);
      rawbar();
    }
    STAMP(17);
    const u32 aW = lb + wl * 8192 + (4 * h2 + q4) * 128 + (((q4 >> 1) & 1) << 6) + (g4 & 1) * 32 + p4s * 8;
    u32 aS = lb + 2 * SLOT + lane * 16, aWc[4][2];
#pragma unroll
    for (int i = 0; i < 4; ++i) {
      aWc[i][0] = aW + ((0x1403u >> (4 * ((i - bp) & 3))) & 7) * SLOT; aWc[i][1] = aWc[i][0] ^ 64;
      asm volatile("" : "+v"(aWc[i][0]), "+v"(aWc[i][1]));
    }
    asm volatile("" : "+v"(aS));
    bf16x8 Wf[2][8];
#define WRD1(i_, n_) Wf[(i_) & 1][n_] = ldstr(aWc[i_][(n_) & 1] + ((n_) >> 1) * 2048, aWc[i_][(n_) & 1] + ((n_) >> 1) * 2048 + 1024)
    waitvm<0>();
#pragma unroll
    for (int n = 0; n < 8; ++n) WRD1(0, n);
    const u32 src = quad * 2 + par, Ld = (bp * 16 + wl * 2) * 4 + s;
#pragma unroll
    for (int i = 0; i < 4; ++i) {
      rawbar();
      if (i == 0) STAMP(20);
      if (i == 3) STAMP(23);
      bf16x8 of[8];
      if (i < 3) {
#pragma unroll
        for (int n = 0; n < 8; ++n) of[n] = ldsb128(aS + i * 8192 + ((n & 1) * 4 + (n >> 1)) * 1024);
        PIN();
#pragma unroll
        for (int n = 0; n < 16; ++n) {
          const int c = n >> 2, rb = (n >> 1) & 1, ct = n & 1;
          if (i == 0 && c == 0) acc[rb * 2 + ct] = f32x16{};
          acc[rb * 2 + ct] = __builtin_amdgcn_mfma_f32_32x32x16_bf16(Wf[i & 1][2 * c + ct], of[2 * c + rb], acc[rb * 2 + ct], 0, 0, 0);
          if (n >= 4 && n < 12) WRD1((i + 1) & 3, n - 4);
          PIN();
        }
      } else {
#pragma unroll
        for (int n = 0; n < 8; ++n) of[n] = ldsb128(aS + i * 8192 + n * 1024);
        PIN();
#pragma unroll
        for (int n = 0; n < 24; ++n) {
          if (n < 16) { const int j = n >> 2, c = n & 3; acc[j] = __builtin_amdgcn_mfma_f32_32x32x16_bf16(Wf[1][2 * c + (j & 1)], of[4 * (j >> 1) + c], acc[j], 0, 0, 0); }
          PIN();
          if (n >= 7 && n < 23 && (n & 1)) {
            const int j = (n - 7) >> 2, t = ((n - 7) >> 1) & 1;
            const u32x4 tv = {cvtpk(acc[j][8 * t], acc[j][8 * t + 1]), cvtpk(acc[j][8 * t + 2], acc[j][8 * t + 3]), cvtpk(acc[j][8 * t + 4], acc[j][8 * t + 5]), cvtpk(acc[j][8 * t + 6], acc[j][8 * t + 7])};
            const u32 so = WS_T + ((Ld + 4 * (j & 1) + 32 * (j >> 1)) * 16 + (TSWZ ? src ^ (wl * 4 + (j & 1) * 2) : src)) * 2048;
            if (t == 0) st16<0>(tv, lane * 16, rw, so); else st16<1024>(tv, lane * 16, rw, so);
          }
          PIN();
        }
      }
    }
#undef WRD1
    STAMP(24);
    waitvm<0>();
    st4(1u, lane < 4 ? ((lane & 1) * 4 + (lane >> 1) * 32) * 1024 : OOB, rw, WS_FLAG + Ld * 1024 + 256 + par * 128 + quad * 4);
    STAMP(25);
    if (w == 0) poll((lane & 7) * 4 + (lane & 8) * 16, rw, WS_FLAG + L * 1024 + 256);
  } else {
    {
      const auto sw = __builtin_amdgcn_permlane32_swap(__float_as_uint(ls), __float_as_uint(ls), false, false);
      ls = __uint_as_float(sw[0]) + __uint_as_float(sw[1]);
    }
    const bool bad = !__all(ls >= 0x1p-40f && ls <= 0x1p40f);
    u32x2 stat = {0u, __float_as_uint(ls)};
    const u32 hs = (ba * 32 + qa * 4 + wl) * 4 + s, pso = WS_P + hs * 8192;
    __builtin_amdgcn_raw_buffer_store_b64(stat, rw, lane < 32 ? lane * 8 : OOB, WS_STAT + hs * 256, 16);
    STAMP(11);
    rawbar();
    STAMP(12);
    prio(1);
#define BRD(n_) ldstr(aV[(n_) >> 4] + ((n_) & 15) * 4096, aV[(n_) >> 4] + ((n_) & 15) * 4096 + 2048)
#define DW(tt_, k_) Dr[2 * (tt_) + ((k_) >> 2)][(k_) & 3]
#pragma unroll
    for (int n = 0; n < PD; ++n) vf[n] = BRD(n);
    PIN();
    u32x4 Dr[8], ov;
    float D3[16], fx, fy;
    SFOR(67, n)
      if constexpr (n < 64) {
        if constexpr ((n & 15) == 0) O[n >> 4] = f32x16{};
        O[n >> 4] = __builtin_amdgcn_mfma_f32_32x32x16_bf16(vf[n % PD], __builtin_bit_cast(bf16x8, P[n & 15]), O[n >> 4], 0, 0, 0);
      }
      if constexpr (n + PD < 64) vf[n % PD] = BRD(n + PD);
      if constexpr (n >= 2 && n < 10 && !(n & 1)) Dr[(n - 2) >> 1] = *(const LAS u32x4*)(size_t)(dump + ((n - 2) >> 1) * 1024);
      if constexpr (n >= 14 && n < 22 && !(n & 1)) Dr[(n - 6) >> 1] = *(const LAS u32x4*)(size_t)(dump + ((n - 6) >> 1) * 1024);
      if constexpr (n >= 19) {
        constexpr int tt = (n - 19) >> 4, k = ((n - 19) & 15) >> 1;
        if constexpr (!((n - 19) & 1)) { fx = pinf(O[tt][2 * k] + bflo(DW(tt, k))); fy = pinf(bfhi(DW(tt, k))); }
        else {
          u32 pk = cvtpk(fx, pinf(O[tt][2 * k + 1] + fy));
          asm volatile("" : "+v"(pk));
          ov[k & 3] = pk;
          if constexpr (k == 3) st16<0>(ov, lane * 16, rw, pso + tt * 2048);
          if constexpr (k == 7) st16<1024>(ov, lane * 16, rw, pso + tt * 2048);
        }
      }
      if constexpr (n >= 30 && n < 62 && !(n & 1)) { constexpr int k = (n - 30) >> 1; D3[k] = pinf(k & 1 ? bfhi(DW(3, k >> 1)) : bflo(DW(3, k >> 1))); }
      PIN();
      if constexpr (n == 13) { rawbar(); PIN(); }
      if constexpr (n == 31) { asm volatile("s_barrier" : "+v"(Dr[4]), "+v"(Dr[5]), "+v"(Dr[6]), "+v"(Dr[7])::"memory"); PIN(); }
      if constexpr (n == 59) { bar(); PIN(); }
    SEND
#pragma unroll
    for (int k = 0; k < 4; ++k) ov[k] = cvtpk(O[3][2 * k] + D3[2 * k], O[3][2 * k + 1] + D3[2 * k + 1]);
    st16<2048>(ov, lane * 16, rw, pso + 4096);
#pragma unroll
    for (int k = 4; k < 8; ++k) ov[k & 3] = cvtpk(O[3][2 * k] + D3[2 * k], O[3][2 * k + 1] + D3[2 * k + 1]);
    st16<3072>(ov, lane * 16, rw, pso + 4096);
#undef BRD
#undef DW
    prio(0);
    STAMP(15);
    if (__builtin_expect(bad, 0)) {
      QLOAD();
      asm volatile("s_waitcnt vmcnt(0)" : QOPS::"memory");
      const GAS char* kp = (const GAS char*)pk + kv0 + r32 * 256 + h2 * 16;
      const GAS unsigned short* vp = (const GAS unsigned short*)((const GAS char*)pv + kv0) + 4 * h2 * 128 + r32;
      float M = -__builtin_inff(), l = 0.f;
      O[0] = f32x16{}; O[1] = f32x16{}; O[2] = f32x16{}; O[3] = f32x16{};
#pragma unroll 1
      for (int it = 0; it < 32; ++it) {
        const int t = it & 15;
        f32x16 a = {};
#pragma unroll
        for (int d = 0; d < 8; ++d) a = __builtin_amdgcn_mfma_f32_32x32x16_bf16(*(const GAS bf16x8*)(kp + t * 8192 + d * 32), __builtin_bit_cast(bf16x8, qf[d]), a, 0, 0, 0);
        if (it < 16) {
#pragma unroll
          for (int i = 0; i < 16; ++i) M = max2f(M, a[i]);
          if (it == 15) {
            const auto sw = __builtin_amdgcn_permlane32_swap(__float_as_uint(M), __float_as_uint(M), false, false);
            M = max2f(__uint_as_float(sw[0]), __uint_as_float(sw[1]));
          }
        } else {
#pragma unroll
          for (int i = 0; i < 16; ++i) { a[i] = __builtin_amdgcn_exp2f(a[i] - M); l += a[i]; }
#pragma unroll
          for (int u = 0; u < 2; ++u) {
            const u32x4 pp = {cvtpk(a[8 * u], a[8 * u + 1]), cvtpk(a[8 * u + 2], a[8 * u + 3]), cvtpk(a[8 * u + 4], a[8 * u + 5]), cvtpk(a[8 * u + 6], a[8 * u + 7])};
#pragma unroll
            for (int dt = 0; dt < 4; ++dt) {
              u32x4 vv;
#pragma unroll
              for (int k = 0; k < 4; ++k) {
                const GAS unsigned short* q_ = vp + (t * 32 + 16 * u + 8 * (k >> 1) + 2 * (k & 1)) * 128 + dt * 32;
                vv[k] = (u32)q_[0] | ((u32)q_[128] << 16);
              }
              O[dt] = __builtin_amdgcn_mfma_f32_32x32x16_bf16(__builtin_bit_cast(bf16x8, vv), __builtin_bit_cast(bf16x8, pp), O[dt], 0, 0, 0);
            }
          }
        }
      }
      const auto sw = __builtin_amdgcn_permlane32_swap(__float_as_uint(l), __float_as_uint(l), false, false);
      l = __uint_as_float(sw[0]) + __uint_as_float(sw[1]);
      stat = u32x2{__float_as_uint(M), __float_as_uint(l)};
#pragma unroll
      for (int f = 0; f < 8; ++f) {
        const int dt = f >> 1, o = (f & 1) * 8;
        st16<0>(u32x4{cvtpk(O[dt][o], O[dt][o + 1]), cvtpk(O[dt][o + 2], O[dt][o + 3]), cvtpk(O[dt][o + 4], O[dt][o + 5]),
                      cvtpk(O[dt][o + 6], O[dt][o + 7])}, lane * 16, rw, pso + f * 1024);
      }
      __builtin_amdgcn_raw_buffer_store_b64(stat, rw, lane < 32 ? lane * 8 : OOB, WS_STAT + hs * 256, 16);
    }
    barvm<0>();
    if (w == 4) st4(1u, lane < 8 ? ((lane >> 2) * 32 + (lane & 3)) * 1024 : OOB, rw, WS_FLAG + (bpa * 64 + qa * 4) * 1024 + 640 + e * 4);
    STAMP(17);
    poll8((const char*)pws, rw, WS_FLAG + L * 1024 + 640, lane);
    STAMP(18);
    const u32 hd = (bp * 2 + (wl >> 1)) * 32 + quad * 4 + par * 2 + (wl & 1);
    u32x2 st[2];
    u32x4 G[5][4];
    const u32 so = WS_P + hd * 32768;
#define GLD(f_, n_) ld16<((f_) & 3) * 1024>(G[(f_) % 5][n_], lane * 16, rw, so + (n_) * 8192 + ((f_) >> 2) * 4096)
#define GLOAD(f_) do { GLD(f_, 0); GLD(f_, 1); GLD(f_, 2); GLD(f_, 3); } while (0)
    ld8<0>(st[0], lane * 8, rw, WS_STAT + hd * 1024); ld8<512>(st[1], lane * 8, rw, WS_STAT + hd * 1024);
    GLOAD(0); GLOAD(1); GLOAD(2);
    asm volatile("s_waitcnt vmcnt(12)" : "+v"(st[0]), "+v"(st[1])::"memory");
    float wt[4];
    {
      const auto m01 = __builtin_amdgcn_permlane32_swap(st[0][0], st[0][0], false, false), m23 = __builtin_amdgcn_permlane32_swap(st[1][0], st[1][0], false, false);
      const auto l01 = __builtin_amdgcn_permlane32_swap(st[0][1], st[0][1], false, false), l23 = __builtin_amdgcn_permlane32_swap(st[1][1], st[1][1], false, false);
      const float mm[4] = {__uint_as_float(m01[0]), __uint_as_float(m01[1]), __uint_as_float(m23[0]), __uint_as_float(m23[1])};
      const float ll[4] = {__uint_as_float(l01[0]), __uint_as_float(l01[1]), __uint_as_float(l23[0]), __uint_as_float(l23[1])};
      const float M = max2f(max2f(mm[0], mm[1]), max2f(mm[2], mm[3]));
      float den = 0.f;
#pragma unroll
      for (int sp = 0; sp < 4; ++sp) { wt[sp] = __builtin_amdgcn_exp2f(mm[sp] - M); den = fmaf(wt[sp], ll[sp], den); }
      const float inv = __builtin_amdgcn_rcpf(den);
      wt[0] *= inv; wt[1] *= inv; wt[2] *= inv; wt[3] *= inv;
      asm volatile("" : "+v"(wt[0]), "+v"(wt[1]), "+v"(wt[2]), "+v"(wt[3]));
    }
    const u32 aS = lb + 2 * SLOT + wl * 2048 + lane * 16;
#define FSTEP(f_, cnt_) do { \
      asm volatile("s_waitcnt vmcnt(" #cnt_ ")" : "+v"(G[(f_) % 5][0]), "+v"(G[(f_) % 5][1]), "+v"(G[(f_) % 5][2]), "+v"(G[(f_) % 5][3])::"memory"); \
      if ((f_) == 0) STAMP(19); \
      u32x4 o; \
      _Pragma("unroll") for (int d = 0; d < 4; ++d) { \
        if ((f_) + 3 < 8) { if (d == 0) GLD(((f_) + 3) & 7, 0); if (d == 1) GLD(((f_) + 3) & 7, 1); if (d == 2) GLD(((f_) + 3) & 7, 2); if (d == 3) GLD(((f_) + 3) & 7, 3); } \
        float lo = bflo(G[(f_) % 5][0][d]) * wt[0], hi = bfhi(G[(f_) % 5][0][d]) * wt[0]; \
        _Pragma("unroll") for (int sp = 1; sp < 4; ++sp) { lo = fmaf(bflo(G[(f_) % 5][sp][d]), wt[sp], lo); hi = fmaf(bfhi(G[(f_) % 5][sp][d]), wt[sp], hi); } \
        o[d] = cvtpk(pinf(lo), pinf(hi)); \
      } \
      *(LAS u32x4*)(size_t)(aS + ((f_) >> 1) * 8192 + ((f_) & 1) * 1024) = o; \
      if ((f_) & 1) bar();     \
    } while (0)
    FSTEP(0, 8); FSTEP(1, 8); STAMP(20); FSTEP(2, 8); FSTEP(3, 8); STAMP(21); FSTEP(4, 8); FSTEP(5, 8); STAMP(22); FSTEP(6, 4); FSTEP(7, 0); STAMP(23);
#undef FSTEP
#undef GLD
#undef GLOAD
  }
  {
    bar();
    STAMP(26);
    u32x4 T[4];
    const u32 vo = g4 * 2048 + w * 256 + l15 * 16, so = WS_T + L * 32768;
#pragma unroll
    for (int i = 0; i < 4; ++i) ld16<0>(T[i], vo, rw, so + i * 8192);
    asm volatile("s_waitcnt vmcnt(0)" : "+v"(T[0]), "+v"(T[1]), "+v"(T[2]), "+v"(T[3])::"memory");
    STAMP(27);
    float v[8];
#pragma unroll
    for (int d = 0; d < 4; ++d) {
      v[2 * d] = (bflo(T[0][d]) + bflo(T[1][d])) + (bflo(T[2][d]) + bflo(T[3][d]));
      v[2 * d + 1] = (bfhi(T[0][d]) + bfhi(T[1][d])) + (bfhi(T[2][d]) + bfhi(T[3][d]));
    }
    float u[4], z[2];
#pragma unroll
    for (int i = 0; i < 4; ++i) {
      const auto sw = __builtin_amdgcn_permlane32_swap(__float_as_uint(v[i]), __float_as_uint(v[i + 4]), false, false);
      u[i] = __uint_as_float(sw[0]) + __uint_as_float(sw[1]);
    }
#pragma unroll
    for (int i = 0; i < 2; ++i) {
      const auto sw = __builtin_amdgcn_permlane16_swap(__float_as_uint(u[i]), __float_as_uint(u[i + 2]), false, false);
      z[i] = __uint_as_float(sw[0]) + __uint_as_float(sw[1]);
    }
    __builtin_amdgcn_raw_buffer_store_b32(cvtpk(z[0], z[1]), rout, l15 * 2048 + g4 * 4,
                                          (b * 32 + (w & 1) * 16) * 2048 + (s * 256 + quad * 32 + (w >> 1) * 8) * 2, 0);
  }
}

extern "C" void kernel_launch(void* const* d_in, const int* in_sizes, int n_in, void* d_out, int out_size, void* d_ws, size_t ws_size,
                              hipStream_t stream) {
  (void)hipFuncSetAttribute((const void*)fused_kernel, hipFuncAttributeMaxDynamicSharedMemorySize, 163840);
  fused_kernel<<<256, 512, 163840, stream>>>(d_in[0], d_in[1], d_in[2], d_in[3], d_in[4], d_out, d_ws);
}
```
